```python
import torch

def _softmax(x, axis=-1):
    return torch.softmax(x, dim=axis)
def _gelu(x):
    return torch.nn.functional.gelu(x, approximate="tanh")
def _silu(x):
    return torch.nn.functional.silu(x)
def _sigmoid(x):
    return torch.sigmoid(x)
def _layernorm(x, axis=-1, eps=1e-5):
    m = x.mean(dim=axis, keepdim=True)
    v = x.var(dim=axis, keepdim=True, unbiased=False)
    return (x - m) / torch.sqrt(v + eps)
def _scatter_add(tgt, idx, src, axis=0):
    return tgt.clone().index_add_(axis, idx, src)
def _segment_sum(src, seg, n):
    out = torch.zeros((n,) + src.shape[1:], dtype=src.dtype)
    return out.index_add_(0, seg, src)
def _pad(x, pw):
    flat = tuple(v for pair in reversed(pw) for v in pair)
    return torch.nn.functional.pad(x, flat)

kernel_name = 'sequential_preLN__ssm_conv_s4__dense_activation_silu__prompt'

def setup_inputs():

    _w = (lambda a: (lambda t: t.to(torch.bfloat16) if t.is_floating_point() else t)(torch.as_tensor(a)))
    def _A_setup():
        B, L, D, N = 1, 2048, 512, 128
        A_re = -(lambda _x: torch.exp(torch.as_tensor(_x)))((lambda *s: torch.randn(*s, dtype=torch.bfloat16) * 6.4)(D, N))
        A_im = torch.pi * (lambda *s: torch.randn(*s, dtype=torch.bfloat16) * 0.05)(D, N)
        B_re = (lambda *s: torch.randn(*s, dtype=torch.bfloat16) * 0.1)(D, N)
        B_im = (lambda *s: torch.randn(*s, dtype=torch.bfloat16) * 0.1)(D, N)
        C_re = (lambda *s: torch.randn(*s, dtype=torch.bfloat16) * 0.1)(D, N)
        C_im = (lambda *s: torch.randn(*s, dtype=torch.bfloat16) * 0.1)(D, N)
        log_dt = (lambda *s: torch.randn(*s, dtype=torch.bfloat16) * 0.05)(D) * 0.5 - 4.0
        Dskip = (lambda *s: torch.randn(*s, dtype=torch.bfloat16) * 0.05)(D)
        return {
            "u": _w((lambda *s: torch.randn(*s, dtype=torch.bfloat16) * 0.05)(B, L, D)),
            "A_re": _w(A_re),
            "A_im": _w(A_im),
            "B_re": _w(B_re),
            "B_im": _w(B_im),
            "C_re": _w(C_re),
            "C_im": _w(C_im),
            "log_dt": _w(log_dt),
            "Dskip": _w(Dskip),
        }
    _A_in = _A_setup()

    def _B_setup():
        B, S, D, F = 1, 2048, 512, 1792
        return {
            "x": _w((lambda *s: torch.randn(*s, dtype=torch.bfloat16) * 0.05)(B, S, D)),
            "w1": _w((lambda *s: torch.randn(*s, dtype=torch.bfloat16) * 0.8)(D, F) * (D ** -0.5)),
            "w2": _w((lambda *s: torch.randn(*s, dtype=torch.bfloat16) * 0.2)(F, D) * (F ** -0.5)),
        }
    _B_in = _B_setup()

    return {
        "A_u": _A_in["u"],
        "A_A_re": _A_in["A_re"],
        "A_A_im": _A_in["A_im"],
        "A_B_re": _A_in["B_re"],
        "A_B_im": _A_in["B_im"],
        "A_C_re": _A_in["C_re"],
        "A_C_im": _A_in["C_im"],
        "A_log_dt": _A_in["log_dt"],
        "A_Dskip": _A_in["Dskip"],
        "B_x": _B_in["x"],
        "B_w1": _B_in["w1"],
        "B_w2": _B_in["w2"],
    }

def reference(A_u, A_A_re, A_A_im, A_B_re, A_B_im, A_C_re, A_C_im, A_log_dt, A_Dskip, B_x, B_w1, B_w2):
    def _A_ref(u, A_re, A_im, B_re, B_im, C_re, C_im, log_dt, Dskip):
        Bsz, L, D = u.shape
        dt = torch.exp(log_dt)[:, None]
        A = A_re + 1j * A_im
        Bc = B_re + 1j * B_im
        Cc = C_re + 1j * C_im
        logdA = dt * A
        dB = dt * Bc
        l = (lambda x, dt: x.to(dt))(torch.arange(L), A_re.dtype)
        P = torch.exp(logdA[:, :, None] * l[None, None, :])
        K = torch.einsum("dn,dnl,dn->dl", Cc, P, dB).real
        L2 = 2 * L
        Kp = _pad(K, ((0, 0), (0, L)))
        up = (lambda x, *d: x.permute(*d))(u, 0, 2, 1)
        up = _pad(up, ((0, 0), (0, 0), (0, L)))
        Kf = (lambda x, axes=(-2,-1): torch.fft.rfft2(x.float(), dim=axes))(Kp[:, None, :], axes=(-2, -1))
        Uf = (lambda x, axes=(-2,-1): torch.fft.rfft2(x.float(), dim=axes))(up[:, :, None, :], axes=(-2, -1))
        Yf = Uf * Kf[None, :, :, :]
        y = (lambda x, s, axes=(-2,-1): torch.fft.irfft2(x, s=s, dim=axes).to(torch.bfloat16))(Yf, (1, L2), axes=(-2, -1))[:, :, 0, :L]
        y = (lambda x, *d: x.permute(*d))(y, 0, 2, 1) + u * Dskip[None, None, :]
        return y

    def _B_ref(x, w1, w2):
        h = torch.einsum("bsd,df->bsf", x, w1)
        h = _silu(h)
        out = torch.einsum("bsf,fd->bsd", h, w2)
        return out

    x = A_u
    h = x + _A_ref(_layernorm(x, axis=-1), A_A_re, A_A_im, A_B_re, A_B_im, A_C_re, A_C_im, A_log_dt, A_Dskip)
    h2 = h + _B_ref(_layernorm(h, axis=-1), B_w1, B_w2)
    return h2
```

```cpp
#include <hip/hip_runtime.h>
#include <hip/hip_bf16.h>
#include <stdint.h>

#define L_ 2048
#define D_ 512
#define N_ 128
#define F_ 1792

__device__ __forceinline__ float bf2f(uint16_t v) { return __uint_as_float(((uint32_t)v) << 16); }
__device__ __forceinline__ uint16_t f2bf(float f) {
  __hip_bfloat16 b = __float2bfloat16(f);
  return *reinterpret_cast<uint16_t*>(&b);
}
__device__ __forceinline__ float rbf(float f) { return bf2f(f2bf(f)); }

__global__ void ln_kernel(const uint16_t* __restrict__ x, uint16_t* __restrict__ out) {
  int row = blockIdx.x * 4 + (threadIdx.x >> 6);
  int lane = threadIdx.x & 63;
  float v[8];
  float s = 0.f;
  for (int j = 0; j < 8; ++j) { v[j] = bf2f(x[row * D_ + lane * 8 + j]); s += v[j]; }
  for (int o = 32; o > 0; o >>= 1) s += __shfl_xor(s, o);
  float mean = s * (1.0f / D_);
  float q = 0.f;
  for (int j = 0; j < 8; ++j) { float d = v[j] - mean; q += d * d; }
  for (int o = 32; o > 0; o >>= 1) q += __shfl_xor(q, o);
  float var = q * (1.0f / D_);
  float m = rbf(mean), vb = rbf(var);
  float sd = rbf(sqrtf(rbf(vb + 1e-5f)));
  for (int j = 0; j < 8; ++j) out[row * D_ + lane * 8 + j] = f2bf(rbf(v[j] - m) / sd);
}

__global__ void kgen_kernel(const uint16_t* Are, const uint16_t* Aim, const uint16_t* Bre, const uint16_t* Bim,
                            const uint16_t* Cre, const uint16_t* Cim, const uint16_t* logdt, float* K) {
  int d = blockIdx.x;
  __shared__ float zx[N_], zy[N_], cr[N_], ci[N_];
  float dt = rbf(expf(bf2f(logdt[d])));
  if (threadIdx.x < N_) {
    int n = threadIdx.x;
    zx[n] = dt * bf2f(Are[d * N_ + n]);
    zy[n] = dt * bf2f(Aim[d * N_ + n]);
    float br = dt * bf2f(Bre[d * N_ + n]), bi = dt * bf2f(Bim[d * N_ + n]);
    float c_r = bf2f(Cre[d * N_ + n]), c_i = bf2f(Cim[d * N_ + n]);
    cr[n] = c_r * br - c_i * bi;
    ci[n] = c_r * bi + c_i * br;
  }
  __syncthreads();
  for (int l = threadIdx.x; l < L_; l += blockDim.x) {
    float lf = rbf((float)l);
    float acc = 0.f;
    for (int n = 0; n < N_; ++n) {
      float e = expf(zx[n] * lf);
      float sn, cs;
      sincosf(zy[n] * lf, &sn, &cs);
      acc += cr[n] * e * cs - ci[n] * e * sn;
    }
    K[d * L_ + l] = acc;
  }
}

__global__ void conv_kernel(const uint16_t* x, const uint16_t* u, const float* K, const uint16_t* Dskip, uint16_t* h) {
  int d = blockIdx.x;
  __shared__ float us[L_], ks[L_];
  for (int l = threadIdx.x; l < L_; l += blockDim.x) { us[l] = bf2f(u[l * D_ + d]); ks[l] = K[d * L_ + l]; }
  __syncthreads();
  float dsk = bf2f(Dskip[d]);
  for (int l = threadIdx.x; l < L_; l += blockDim.x) {
    float acc = 0.f;
    for (int t = 0; t <= l; ++t) acc += ks[t] * us[l - t];
    float y = rbf(acc);
    float r = rbf(y + rbf(us[l] * dsk));
    h[l * D_ + d] = f2bf(bf2f(x[l * D_ + d]) + r);
  }
}

__global__ void gemm_kernel(const uint16_t* A, const uint16_t* B, uint16_t* C, const uint16_t* res, int M, int N, int K, int mode) {
  int n = blockIdx.x * 64 + (threadIdx.x & 63);
  int m = blockIdx.y * 4 + (threadIdx.x >> 6);
  float acc = 0.f;
  for (int k = 0; k < K; ++k) acc += bf2f(A[m * K + k]) * bf2f(B[k * N + n]);
  float c = rbf(acc);
  if (mode == 0) {
    float s = c / (1.0f + expf(-c));
    C[m * N + n] = f2bf(s);
  } else {
    C[m * N + n] = f2bf(bf2f(res[m * N + n]) + c);
  }
}

extern "C" void kernel_launch(void* const* d_in, const int* in_sizes, int n_in,
                              void* d_out, int out_size, void* d_ws, size_t ws_size,
                              hipStream_t stream) {
  const uint16_t* x = (const uint16_t*)d_in[0];
  const uint16_t* Are = (const uint16_t*)d_in[1];
  const uint16_t* Aim = (const uint16_t*)d_in[2];
  const uint16_t* Bre = (const uint16_t*)d_in[3];
  const uint16_t* Bim = (const uint16_t*)d_in[4];
  const uint16_t* Cre = (const uint16_t*)d_in[5];
  const uint16_t* Cim = (const uint16_t*)d_in[6];
  const uint16_t* logdt = (const uint16_t*)d_in[7];
  const uint16_t* Dskip = (const uint16_t*)d_in[8];
  const uint16_t* w1 = (const uint16_t*)d_in[10];
  const uint16_t* w2 = (const uint16_t*)d_in[11];
  uint16_t* out = (uint16_t*)d_out;
  char* ws = (char*)d_ws;
  uint16_t* u = (uint16_t*)(ws);
  float* K = (float*)(ws + (2 << 20));
  uint16_t* h = (uint16_t*)(ws + (6 << 20));
  uint16_t* a2 = (uint16_t*)(ws + (8 << 20));
  uint16_t* h1 = (uint16_t*)(ws + (10 << 20));
  ln_kernel<<<L_ / 4, 256, 0, stream>>>(x, u);
  kgen_kernel<<<D_, 256, 0, stream>>>(Are, Aim, Bre, Bim, Cre, Cim, logdt, K);
  conv_kernel<<<D_, 256, 0, stream>>>(x, u, K, Dskip, h);
  ln_kernel<<<L_ / 4, 256, 0, stream>>>(h, a2);
  gemm_kernel<<<dim3(F_ / 64, L_ / 4), 256, 0, stream>>>(a2, w1, h1, nullptr, L_, F_, D_, 0);
  gemm_kernel<<<dim3(D_ / 64, L_ / 4), 256, 0, stream>>>(h1, w2, out, h, L_, D_, F_, 1);
}
```

```cpp
#include <hip/hip_runtime.h>
#include <hip/hip_bf16.h>
#include <stdint.h>

constexpr int L_ = 2048, D_ = 512, N_ = 128, F_ = 1792;

typedef __attribute__((ext_vector_type(8))) short bf16x8;
typedef __attribute__((ext_vector_type(4))) short bf16x4;
typedef __attribute__((ext_vector_type(4))) float f32x4;
typedef __attribute__((ext_vector_type(2))) float f32x2;
typedef __attribute__((ext_vector_type(16))) float f32x16;
typedef __attribute__((ext_vector_type(4))) unsigned u32x4;
typedef __attribute__((ext_vector_type(2))) unsigned u32x2;
typedef __attribute__((ext_vector_type(2))) __bf16 bf16x2_t;
typedef __attribute__((address_space(3))) bf16x4 lds_bf16x4;
typedef __attribute__((address_space(3))) void lds_void;
typedef __attribute__((address_space(1))) const void glb_cvoid;

__device__ __forceinline__ float bf2f(uint16_t v) { return __uint_as_float(((uint32_t)v) << 16); }
__device__ __forceinline__ float bflo(uint32_t v) { return __uint_as_float(v << 16); }
__device__ __forceinline__ float bfhi(uint32_t v) { return __uint_as_float(v & 0xffff0000u); }
__device__ __forceinline__ uint16_t f2bf(float f) {
  __hip_bfloat16 b = __float2bfloat16(f);
  return *reinterpret_cast<uint16_t*>(&b);
}
__device__ __forceinline__ float rbf(float f) { return bf2f(f2bf(f)); }
__device__ __forceinline__ uint32_t pack2(float lo, float hi) { return (uint32_t)f2bf(lo) | ((uint32_t)f2bf(hi) << 16); }

__device__ __forceinline__ void glds16(const void* g, void* l) {
  __builtin_amdgcn_global_load_lds((glb_cvoid*)g, (lds_void*)l, 16, 0, 0);
}
#define WAIT_VM(n) asm volatile("s_waitcnt vmcnt(" #n ")" ::: "memory")
#define WAIT_LGKM0() asm volatile("s_waitcnt lgkmcnt(0)" ::: "memory")
#define RAW_BARRIER() do { asm volatile("" ::: "memory"); __builtin_amdgcn_s_barrier(); asm volatile("" ::: "memory"); } while (0)

constexpr size_t WS_UT = 0;
constexpr size_t WS_XT = WS_UT + (size_t)D_ * L_ * 2;
constexpr size_t WS_HT = WS_XT + (size_t)D_ * L_ * 2;
constexpr size_t WS_KG = WS_HT + (size_t)D_ * L_ * 2;
constexpr int KG_CH = 8 * 80 * 8;
constexpr size_t WS_CS = WS_KG + (size_t)D_ * KG_CH * 2;
constexpr size_t WS_H1 = WS_CS + 8192;

constexpr int K1_LN_BLOCKS = 128;
constexpr int K1_CS_BLOCKS = 32;
constexpr int K1_KG_BASE = K1_LN_BLOCKS + K1_CS_BLOCKS;
constexpr int KS_ = 260;
constexpr int K1_LDS = (48 + 16) * KS_ * 4;

struct cplx { float r, i; };
__device__ __forceinline__ cplx cmul(cplx a, cplx b) {
  cplx o; o.r = a.r * b.r - a.i * b.i; o.i = a.r * b.i + a.i * b.r; return o;
}
__device__ __forceinline__ cplx cexp_(float x, float y) {
  float m = expf(x), s, c; sincosf(y, &s, &c);
  cplx o; o.r = m * c; o.i = m * s; return o;
}

__global__ __launch_bounds__(256) void k1_prep(
    const uint16_t* __restrict__ x, const uint16_t* __restrict__ Are, const uint16_t* __restrict__ Aim,
    const uint16_t* __restrict__ Bre, const uint16_t* __restrict__ Bim, const uint16_t* __restrict__ Cre,
    const uint16_t* __restrict__ Cim, const uint16_t* __restrict__ logdt, const uint16_t* __restrict__ w1,
    char* __restrict__ ws) {
  extern __shared__ __attribute__((aligned(16))) char smem[];
  const int tid = threadIdx.x, lane = tid & 63, wv = tid >> 6;
  const int bid = blockIdx.x;
  if (bid >= K1_KG_BASE) {
    const int d = bid - K1_KG_BASE;
    float* Atab = (float*)smem;
    float* Btab = Atab + 48 * KS_;
    const int n = tid & 127, part = tid >> 7;
    const float dt = rbf(expf(bf2f(logdt[d])));
    const float zx = dt * bf2f(Are[d * N_ + n]);
    const float zy = dt * bf2f(Aim[d * N_ + n]);
    cplx c;
    {
      float br = dt * bf2f(Bre[d * N_ + n]), bi = dt * bf2f(Bim[d * N_ + n]);
      float cr = bf2f(Cre[d * N_ + n]), ci = bf2f(Cim[d * N_ + n]);
      c.r = cr * br - ci * bi; c.i = cr * bi + ci * br;
    }
    auto putA = [&](int row, cplx v) { *(f32x2*)(Atab + row * KS_ + 2 * n) = f32x2{v.r, -v.i}; };
    auto putB = [&](int row, cplx v) { *(f32x2*)(Btab + row * KS_ + 2 * n) = f32x2{v.r, v.i}; };
    const cplx g1 = cexp_(128.f * zx, 128.f * zy);
    if (part == 0) {
      const cplx e1 = cexp_(zx, zy);
      const cplx e8 = cexp_(8.f * zx, 8.f * zy);
      cplx lo; lo.r = 1.f; lo.i = 0.f;
      putB(0, lo);
      lo = e8; putB(1, lo);
#pragma unroll
      for (int b = 2; b < 16; ++b) { lo = cmul(lo, e8); putB(b, lo); }
      cplx ce = c;
#pragma unroll
      for (int e = 0; e < 8; ++e) {
        putA(e, ce);
        putA(8 + e, cmul(ce, g1));
        if (e < 7) ce = cmul(ce, e1);
      }
    } else {
      const cplx e2 = cexp_(2.f * zx, 2.f * zy);
      const cplx c2 = cmul(c, e2), c4 = cmul(c2, e2), c6 = cmul(c4, e2);
      cplx g = cmul(g1, g1);
#pragma unroll
      for (int a = 2; a < 4; ++a) {
        putA(16 + 4 * (a - 2) + 0, cmul(c, g));
        putA(16 + 4 * (a - 2) + 1, cmul(c2, g));
        putA(16 + 4 * (a - 2) + 2, cmul(c4, g));
        putA(16 + 4 * (a - 2) + 3, cmul(c6, g));
        g = cmul(g, g1);
      }
#pragma unroll
      for (int a = 4; a < 8; ++a) {
        putA(24 + 2 * (a - 4) + 0, cmul(c, g));
        putA(24 + 2 * (a - 4) + 1, cmul(c4, g));
        g = cmul(g, g1);
      }
#pragma unroll
      for (int a = 8; a < 17; ++a) {
        putA(32 + (a - 8), cmul(c, g));
        if (a < 16) g = cmul(g, g1);
      }
    }
    __syncthreads();
    f32x4 acc[3] = {f32x4{0, 0, 0, 0}, f32x4{0, 0, 0, 0}, f32x4{0, 0, 0, 0}};
    {
      const int r16 = lane & 15, kk = lane >> 4;
#pragma unroll
      for (int gi = 0; gi < 4; ++gi) {
        const int k0 = (wv * 4 + gi) * 16 + 4 * kk;
        f32x4 bv = *(const f32x4*)(Btab + r16 * KS_ + k0);
        f32x4 av[3];
#pragma unroll
        for (int r = 0; r < 3; ++r) av[r] = *(const f32x4*)(Atab + (16 * r + r16) * KS_ + k0);
#pragma unroll
        for (int t = 0; t < 4; ++t)
#pragma unroll
          for (int r = 0; r < 3; ++r)
            acc[r] = __builtin_amdgcn_mfma_f32_16x16x4f32(av[r][t], bv[t], acc[r], 0, 0, 0);
      }
    }
    __syncthreads();
    float* Kv = (float*)smem;
    {
      const int col = lane & 15, rq = lane >> 4;
#pragma unroll
      for (int r = 0; r < 3; ++r)
#pragma unroll
        for (int j = 0; j < 4; ++j) Kv[wv * 768 + (16 * r + 4 * rq + j) * 16 + col] = acc[r][j];
    }
    __syncthreads();
    uint16_t* Kg = (uint16_t*)(ws + WS_KG) + (size_t)d * KG_CH;
    {
      const int cc = tid & 63, ks = (tid >> 6) & 1, h = tid >> 7;
      uint32_t hi[4], lo[4];
#pragma unroll
      for (int j2 = 0; j2 < 4; ++j2) {
        float kv[2];
#pragma unroll
        for (int s = 0; s < 2; ++s) {
          const int tt = 32 * cc + 16 * ks + 15 - 8 * h - (2 * j2 + s);
          const int lb = (int)rbf((float)tt);
          const int b = (lb >> 3) & 15;
          int row;
          if (lb < 256) row = ((lb >> 7) << 3) + (lb & 7);
          else if (lb < 512) row = 16 + (((lb - 256) >> 7) << 2) + ((lb & 7) >> 1);
          else if (lb < 1024) row = 24 + (((lb - 512) >> 7) << 1) + ((lb & 7) >> 2);
          else row = 32 + ((lb - 1024) >> 7);
          const int ix = row * 16 + b;
          kv[s] = (Kv[ix] + Kv[768 + ix]) + (Kv[1536 + ix] + Kv[2304 + ix]);
        }
        const float h0 = rbf(kv[0]), h1 = rbf(kv[1]);
        hi[j2] = pack2(h0, h1);
        lo[j2] = pack2(kv[0] - h0, kv[1] - h1);
      }
      *(u32x4*)(Kg + ((0 * 2 + ks) * 2 + h) * 640 + (cc + 16) * 8) = u32x4{hi[0], hi[1], hi[2], hi[3]};
      *(u32x4*)(Kg + ((1 * 2 + ks) * 2 + h) * 640 + (cc + 16) * 8) = u32x4{lo[0], lo[1], lo[2], lo[3]};
      if (tid < 128) *(u32x4*)(Kg + (tid >> 4) * 640 + (tid & 15) * 8) = u32x4{0, 0, 0, 0};
    }
  } else if (bid < K1_LN_BLOCKS) {
    constexpr int TS = 1024 + 16;
    const int l0 = bid * 16;
    u32x4 xr[4];
#pragma unroll
    for (int i = 0; i < 4; ++i) xr[i] = *(const u32x4*)(x + (size_t)(l0 + wv * 4 + i) * D_ + lane * 8);
#pragma unroll
    for (int i = 0; i < 4; ++i) {
      float v[8];
#pragma unroll
      for (int j = 0; j < 4; ++j) { v[2 * j] = bflo(xr[i][j]); v[2 * j + 1] = bfhi(xr[i][j]); }
      float s = 0.f;
#pragma unroll
      for (int j = 0; j < 8; ++j) s += v[j];
#pragma unroll
      for (int o = 32; o > 0; o >>= 1) s += __shfl_xor(s, o);
      const float mean = s * (1.0f / D_);
      float q = 0.f;
#pragma unroll
      for (int j = 0; j < 8; ++j) { float dd = v[j] - mean; q += dd * dd; }
#pragma unroll
      for (int o = 32; o > 0; o >>= 1) q += __shfl_xor(q, o);
      const float m = rbf(mean), vb = rbf(q * (1.0f / D_));
      const float sd = rbf(sqrtf(rbf(vb + 1e-5f)));
      u32x4 o;
#pragma unroll
      for (int j = 0; j < 4; ++j) o[j] = pack2(rbf(v[2 * j] - m) / sd, rbf(v[2 * j + 1] - m) / sd);
      *(u32x4*)(smem + (wv * 4 + i) * TS + lane * 16) = o;
    }
#pragma unroll
    for (int pass = 0; pass < 2; ++pass) {
      __syncthreads();
      uint32_t v[16];
#pragma unroll
      for (int r = 0; r < 16; ++r) v[r] = *(const uint32_t*)(smem + r * TS + tid * 4);
      uint16_t* dst = (uint16_t*)(ws + (pass == 0 ? WS_UT : WS_XT));
      u32x4 a0, a1, b0, b1;
#pragma unroll
      for (int j = 0; j < 4; ++j) {
        a0[j] = (v[2 * j] & 0xffffu) | (v[2 * j + 1] << 16);
        b0[j] = (v[2 * j] >> 16) | (v[2 * j + 1] & 0xffff0000u);
        a1[j] = (v[8 + 2 * j] & 0xffffu) | (v[8 + 2 * j + 1] << 16);
        b1[j] = (v[8 + 2 * j] >> 16) | (v[8 + 2 * j + 1] & 0xffff0000u);
      }
      uint16_t* p0 = dst + (size_t)(2 * tid) * L_ + l0;
      *(u32x4*)(p0) = a0; *(u32x4*)(p0 + 8) = a1;
      *(u32x4*)(p0 + L_) = b0; *(u32x4*)(p0 + L_ + 8) = b1;
      if (pass == 0) {
        __syncthreads();
#pragma unroll
        for (int i = 0; i < 4; ++i) *(u32x4*)(smem + (wv * 4 + i) * TS + lane * 16) = xr[i];
      }
    }
  } else {
    const int cb = bid - K1_LN_BLOCKS;
    if (cb >= F_ / 64) return;
    const int cp = tid & 31, rg = tid >> 5;
    float s0 = 0.f, s1 = 0.f;
#pragma unroll 8
    for (int r = 0; r < 64; ++r) {
      uint32_t v = *(const uint32_t*)(w1 + (size_t)(rg * 64 + r) * F_ + cb * 64 + cp * 2);
      s0 += bflo(v); s1 += bfhi(v);
    }
    float* red = (float*)smem;
    red[rg * 64 + cp * 2] = s0; red[rg * 64 + cp * 2 + 1] = s1;
    __syncthreads();
    if (tid < 64) {
      float s = 0.f;
#pragma unroll
      for (int g = 0; g < 8; ++g) s += red[g * 64 + tid];
      ((float*)(ws + WS_CS))[cb * 64 + tid] = s;
    }
  }
}

constexpr int UC_STRIDE = 4352;
constexpr int UREG = 34816;
constexpr int CH_LDS = UREG + 10240;
constexpr int K2_LDS = 2 * CH_LDS;

__global__ __launch_bounds__(256) void k2_conv(const uint16_t* __restrict__ Dskip, char* __restrict__ ws) {
  extern __shared__ __attribute__((aligned(16))) char smem[];
  const int tid = threadIdx.x, lane = tid & 63;
  const int wv = __builtin_amdgcn_readfirstlane(tid >> 6);
  const int chs = wv >> 1, half = wv & 1;
  const int bid = blockIdx.x;
  const int d = (bid & 7) + 8 * (2 * (bid >> 3) + chs);
  char* cb = smem + chs * CH_LDS;
  const uint16_t* uT = (const uint16_t*)(ws + WS_UT) + (size_t)d * L_;
  const uint16_t* xT = (const uint16_t*)(ws + WS_XT) + (size_t)d * L_;
  const uint16_t* Kg = (const uint16_t*)(ws + WS_KG) + (size_t)d * KG_CH;
  const int tp = tid & 127;
#pragma unroll
  for (int i = 0; i < 5; ++i) {
    const int ii = half * 5 + i;
    glds16(Kg + ii * 512 + lane * 8, cb + UREG + ii * 1024);
  }
#pragma unroll
  for (int it = 0; it < 3; ++it) {
    const int m = it * 128 + tp;
    if (m < 260) {
      const int mp = m - 4;
      u32x4 A = u32x4{0, 0, 0, 0}, B = u32x4{0, 0, 0, 0};
      if (mp >= 0) A = *(const u32x4*)(uT + mp * 8);
      if (mp + 1 >= 0 && mp + 1 < 256) B = *(const u32x4*)(uT + mp * 8 + 8);
      uint32_t S[8] = {A[0], A[1], A[2], A[3], B[0], B[1], B[2], B[3]};
      const int beta[8] = {11, 0, 4, 8, 12, 0, 4, 8};
#pragma unroll
      for (int sg = 0; sg < 8; ++sg) {
        u32x4 o;
        const int s = sg >> 1;
#pragma unroll
        for (int i = 0; i < 4; ++i)
          o[i] = (sg & 1) ? __builtin_amdgcn_alignbit(S[s + i + 1], S[s + i], 16) : S[s + i];
        *(u32x4*)(cb + sg * UC_STRIDE + beta[sg] * 16 + m * 16) = o;
      }
    }
  }
  const u32x4 xe0 = *(const u32x4*)(xT + tp * 16), xe1 = *(const u32x4*)(xT + tp * 16 + 8);
  const u32x4 ue0 = *(const u32x4*)(uT + tp * 16), ue1 = *(const u32x4*)(uT + tp * 16 + 8);
  const float dsk = bf2f(Dskip[d]);
  WAIT_VM(0);
  __syncthreads();

  const int p = lane & 31, h = lane >> 5;
  const int sg = (p + 1) & 7, pi = (p + 1) >> 3;
  const int betal = (sg == 0) ? 11 : (((sg - 1) & 3) * 4);
  const char* abase = cb + sg * UC_STRIDE + betal * 16 + 16 * h + 16 * pi + 32 - 32 * half;
  const char* bbase = cb + UREG + (((p >> 4) * 2 + half) * 2 + h) * 1280 + 16 * ((p & 15) + 16);
  f32x16 acc[4];
#pragma unroll
  for (int t = 0; t < 4; ++t)
#pragma unroll
    for (int j = 0; j < 16; ++j) acc[t][j] = 0.f;
#pragma unroll
  for (int a0 = 0; a0 < 16; ++a0) {
    bf16x8 af[4], bfr[4];
#pragma unroll
    for (int al = 0; al < 4; ++al) af[al] = *(const bf16x8*)(abase + 64 * a0 + 1024 * al);
#pragma unroll
    for (int e = 0; e < 4; ++e) bfr[e] = *(const bf16x8*)(bbase + 256 * e - 16 * a0);
#pragma unroll
    for (int T = 0; T < 4; ++T)
#pragma unroll
      for (int al = 0; al <= T; ++al)
        acc[T] = __builtin_amdgcn_mfma_f32_32x32x16_bf16(af[al], bfr[T - al], acc[T], 0, 0, 0);
  }
  __syncthreads();
  {
    char* pb = cb + (half * 2 + (p >> 4)) * 9216;
#pragma unroll
    for (int T = 0; T < 4; ++T) {
      const int q = 16 * T + (p & 15);
#pragma unroll
      for (int g = 0; g < 4; ++g)
        *(f32x4*)(pb + (q * 36 + 8 * g + 4 * h) * 4) =
            f32x4{acc[T][4 * g], acc[T][4 * g + 1], acc[T][4 * g + 2], acc[T][4 * g + 3]};
    }
  }
  __syncthreads();
  {
    const char* rb = cb + ((tp >> 1) * 36 + 16 * (tp & 1)) * 4;
    uint32_t out[8];
#pragma unroll
    for (int i = 0; i < 4; ++i) {
      f32x4 s = *(const f32x4*)(rb + 16 * i);
      f32x4 s1 = *(const f32x4*)(rb + 9216 + 16 * i);
      f32x4 s2 = *(const f32x4*)(rb + 2 * 9216 + 16 * i);
      f32x4 s3 = *(const f32x4*)(rb + 3 * 9216 + 16 * i);
#pragma unroll
      for (int j2 = 0; j2 < 2; ++j2) {
        const int w = 2 * i + j2;
        const uint32_t xv = (w < 4) ? xe0[w & 3] : xe1[w & 3];
        const uint32_t uv = (w < 4) ? ue0[w & 3] : ue1[w & 3];
        float y0 = rbf((s[2 * j2] + s2[2 * j2]) + (s1[2 * j2] + s3[2 * j2]));
        float y1 = rbf((s[2 * j2 + 1] + s2[2 * j2 + 1]) + (s1[2 * j2 + 1] + s3[2 * j2 + 1]));
        float r0 = rbf(y0 + rbf(bflo(uv) * dsk));
        float r1 = rbf(y1 + rbf(bfhi(uv) * dsk));
        out[w] = pack2(bflo(xv) + r0, bfhi(xv) + r1);
      }
    }
    uint16_t* hT = (uint16_t*)(ws + WS_HT) + (size_t)d * L_ + tp * 16;
    *(u32x4*)(hT) = u32x4{out[0], out[1], out[2], out[3]};
    *(u32x4*)(hT + 8) = u32x4{out[4], out[5], out[6], out[7]};
  }
}

constexpr int G1_HS = 64 * 256;
constexpr int G1_WS = 64 * 224;
constexpr int G1_STAGE = G1_HS + G1_WS;
constexpr int G1_NS = 5;
constexpr int K3_LDS = G1_NS * G1_STAGE;

__device__ __forceinline__ bf16x4 tr_read(const char* p) {
  return __builtin_amdgcn_ds_read_tr16_b64_v4i16((lds_bf16x4*)p);
}
__device__ __forceinline__ bf16x8 cat8(bf16x4 a, bf16x4 b) {
  return bf16x8{a[0], a[1], a[2], a[3], b[0], b[1], b[2], b[3]};
}

__global__ __launch_bounds__(256) void k3_gemm1(const uint16_t* __restrict__ w1, char* __restrict__ ws) {
  extern __shared__ __attribute__((aligned(16))) char smem[];
  const int tid = threadIdx.x, lane = tid & 63;
  const int wv = __builtin_amdgcn_readfirstlane(tid >> 6);
  const int bid = blockIdx.x;
  const int xcd = bid & 7, idx = bid >> 3;
  const int lt = 2 * xcd + (idx & 1), ft = idx >> 1;
  const int l0 = lt * 128, f0 = ft * 112;
  const uint16_t* hT = (const uint16_t*)(ws + WS_HT);
  int hoff[4];
#pragma unroll
  for (int i = 0; i < 4; ++i) {
    const int row = (wv * 4 + i) * 4 + (lane >> 4);
    hoff[i] = row * L_ + l0 + 8 * ((lane & 15) ^ (2 * (row & 7)));
  }
  const int nw = (wv < 2) ? 4 : 3;
  const int wi0 = (wv < 2) ? wv * 4 : 8 + (wv - 2) * 3;
  int woff[4];
#pragma unroll
  for (int i = 0; i < 4; ++i) {
    const int nn = (wi0 + (i < nw ? i : 0)) * 64 + lane;
    const int row = nn / 14, ch = nn - row * 14;
    woff[i] = row * F_ + f0 + 8 * ch;
  }
  auto stage = [&](int kt) {
    char* sb = smem + (kt % G1_NS) * G1_STAGE;
    const uint16_t* hs = hT + (size_t)kt * 64 * L_;
    const uint16_t* wsrc = w1 + (size_t)kt * 64 * F_;
#pragma unroll
    for (int i = 0; i < 4; ++i) glds16(hs + hoff[i], sb + (wv * 4 + i) * 1024);
#pragma unroll
    for (int i = 0; i < 3; ++i) glds16(wsrc + woff[i], sb + G1_HS + (wi0 + i) * 1024);
    if (wv < 2) glds16(wsrc + woff[3], sb + G1_HS + (wi0 + 3) * 1024);
  };
#pragma unroll
  for (int s = 0; s < G1_NS; ++s) stage(s);

  const int g = lane >> 4, q = (lane >> 2) & 3, pp = lane & 3;
  const int xv = 2 * (4 * (g & 1) + q);
  int hrd[2];
#pragma unroll
  for (int cc = 0; cc < 2; ++cc)
    hrd[cc] = (4 * g + q) * 256 + ((((2 * (2 * wv + cc)) ^ xv) + (pp >> 1)) * 16) + 8 * (pp & 1);
  const int wrd = G1_HS + (4 * g + q) * 224 + 8 * pp;

  f32x4 acc[7][2];
#pragma unroll
  for (int i = 0; i < 7; ++i) { acc[i][0] = f32x4{0, 0, 0, 0}; acc[i][1] = f32x4{0, 0, 0, 0}; }
  float ssum[2] = {0.f, 0.f}, ssq[2] = {0.f, 0.f};
  const bf16x2_t ones = __builtin_bit_cast(bf16x2_t, 0x3f803f80u);

#pragma unroll
  for (int kt = 0; kt < 8; ++kt) {
    constexpr int dummy = 0; (void)dummy;
    {
      const int issued = (G1_NS + kt < 8) ? (G1_NS + kt) : 8;
      const int rem = issued - kt - 1;
      if (wv < 2) {
        if (rem >= 4) WAIT_VM(32); else if (rem == 3) WAIT_VM(24); else if (rem == 2) WAIT_VM(16); else if (rem == 1) WAIT_VM(8); else WAIT_VM(0);
      } else {
        if (rem >= 4) WAIT_VM(28); else if (rem == 3) WAIT_VM(21); else if (rem == 2) WAIT_VM(14); else if (rem == 1) WAIT_VM(7); else WAIT_VM(0);
      }
    }
    RAW_BARRIER();
    const char* sb = smem + (kt % G1_NS) * G1_STAGE;
#pragma unroll
    for (int ks = 0; ks < 2; ++ks) {
      bf16x8 hf[2], wf[7];
#pragma unroll
      for (int cc = 0; cc < 2; ++cc)
        hf[cc] = cat8(tr_read(sb + hrd[cc] + ks * 8192), tr_read(sb + hrd[cc] + ks * 8192 + 4096));
#pragma unroll
      for (int ci = 0; ci < 7; ++ci)
        wf[ci] = cat8(tr_read(sb + wrd + ks * 32 * 224 + 32 * ci), tr_read(sb + wrd + (ks * 32 + 16) * 224 + 32 * ci));
#pragma unroll
      for (int ci = 0; ci < 7; ++ci)
#pragma unroll
        for (int cc = 0; cc < 2; ++cc)
          acc[ci][cc] = __builtin_amdgcn_mfma_f32_16x16x32_bf16(wf[ci], hf[cc], acc[ci][cc], 0, 0, 0);
#pragma unroll
      for (int cc = 0; cc < 2; ++cc) {
        const u32x4 hv = __builtin_bit_cast(u32x4, hf[cc]);
#pragma unroll
        for (int j = 0; j < 4; ++j) {
          const uint32_t hw = hv[j];
          const bf16x2_t v2 = __builtin_bit_cast(bf16x2_t, hw);
          ssum[cc] = __builtin_amdgcn_fdot2_f32_bf16(v2, ones, ssum[cc], false);
          ssq[cc] = __builtin_amdgcn_fdot2_f32_bf16(v2, v2, ssq[cc], false);
        }
      }
    }
    if (kt + G1_NS < 8) {
      WAIT_LGKM0();
      RAW_BARRIER();
      stage(kt + G1_NS);
    }
  }
  const float* cs = (const float*)(ws + WS_CS);
  uint16_t* H1 = (uint16_t*)(ws + WS_H1);
#pragma unroll
  for (int cc = 0; cc < 2; ++cc) {
    float s = ssum[cc], sq = ssq[cc];
    s += __shfl_xor(s, 16); s += __shfl_xor(s, 32);
    sq += __shfl_xor(sq, 16); sq += __shfl_xor(sq, 32);
    const float mean = s * (1.0f / D_);
    const float var = fmaxf(sq * (1.0f / D_) - mean * mean, 0.f);
    const float m = rbf(mean);
    const float sd = rbf(sqrtf(rbf(rbf(var) + 1e-5f)));
    const float rs = 1.0f / sd;
    const int l = l0 + 16 * (2 * wv + cc) + (lane & 15);
#pragma unroll
    for (int ci = 0; ci < 7; ++ci) {
      const int f = f0 + 16 * ci + 4 * g;
      const f32x4 c4 = *(const f32x4*)(cs + f);
      float o[4];
#pragma unroll
      for (int j = 0; j < 4; ++j) {
        const float v = rbf((acc[ci][cc][j] - m * c4[j]) * rs);
        o[j] = v / (1.0f + __expf(-v));
      }
      *(u32x2*)(H1 + (size_t)l * F_ + f) = u32x2{pack2(o[0], o[1]), pack2(o[2], o[3])};
    }
  }
}

constexpr int G2_HS = 64 * 256;
constexpr int G2_WS = 128 * 128;
constexpr int G2_STAGE = G2_HS + G2_WS;
constexpr int G2_NS = 4;
constexpr int G2_NK = F_ / 128;
constexpr int K4_LDS = G2_NS * G2_STAGE;

__global__ __launch_bounds__(256) void k4_gemm2(const uint16_t* __restrict__ w2, char* __restrict__ ws,
                                                uint16_t* __restrict__ out) {
  extern __shared__ __attribute__((aligned(16))) char smem[];
  const int tid = threadIdx.x, lane = tid & 63;
  const int wv = __builtin_amdgcn_readfirstlane(tid >> 6);
  const int bid = blockIdx.x;
  const int xcd = bid & 7, idx = bid >> 3;
  const int lt = 4 * xcd + (idx & 3), dtile = idx >> 2;
  const int l0 = lt * 64, d0 = dtile * 64;
  const uint16_t* H1 = (const uint16_t*)(ws + WS_H1);
  const uint16_t* hT = (const uint16_t*)(ws + WS_HT);
  const int g = lane >> 4, q = (lane >> 2) & 3, pp = lane & 3;
  const int wd = wv & 1, wl = wv >> 1;
  uint16_t hres[2][2][4];
#pragma unroll
  for (int ci = 0; ci < 2; ++ci)
#pragma unroll
    for (int cc = 0; cc < 2; ++cc)
#pragma unroll
      for (int j = 0; j < 4; ++j)
        hres[ci][cc][j] = hT[(size_t)(d0 + 16 * (2 * wd + ci) + 4 * g + j) * L_ + l0 + 16 * (2 * wl + cc) + (lane & 15)];
  int hoff[4], woff[4];
#pragma unroll
  for (int i = 0; i < 4; ++i) {
    const int row = (wv * 4 + i) * 4 + (lane >> 4);
    hoff[i] = (l0 + row) * F_ + 8 * ((lane & 15) ^ (row & 15));
    const int r2 = (wv * 4 + i) * 8 + (lane >> 3);
    const int gf = ((r2 >> 1) & 1) | (((r2 >> 3) & 1) << 1);
    woff[i] = r2 * D_ + d0 + 8 * ((lane & 7) ^ (2 * gf));
  }
  auto stage = [&](int kt) {
    char* sb = smem + (kt % G2_NS) * G2_STAGE;
#pragma unroll
    for (int i = 0; i < 4; ++i) glds16(H1 + hoff[i] + kt * 128, sb + (wv * 4 + i) * 1024);
#pragma unroll
    for (int i = 0; i < 4; ++i) glds16(w2 + woff[i] + (size_t)kt * 128 * D_, sb + G2_HS + (wv * 4 + i) * 1024);
  };
#pragma unroll
  for (int s = 0; s < G2_NS; ++s) stage(s);

  int hrd[2];
#pragma unroll
  for (int cc = 0; cc < 2; ++cc) {
    const int row = 16 * (2 * wl + cc) + (lane & 15);
    hrd[cc] = row * 256;
  }
  const int r15 = lane & 15;
  int wrd[2][2];
#pragma unroll
  for (int ci = 0; ci < 2; ++ci)
#pragma unroll
    for (int t = 0; t < 2; ++t) {
      const int row = 8 * g + 4 * t + q;
      const int gf = ((row >> 1) & 1) | (((row >> 3) & 1) << 1);
      wrd[ci][t] = G2_HS + row * 128 + (((2 * (2 * wd + ci)) ^ (2 * gf)) + (pp >> 1)) * 16 + 8 * (pp & 1);
    }
  f32x4 acc[2][2];
#pragma unroll
  for (int i = 0; i < 2; ++i) { acc[i][0] = f32x4{0, 0, 0, 0}; acc[i][1] = f32x4{0, 0, 0, 0}; }

  for (int kt = 0; kt < G2_NK; ++kt) {
    if (kt + 3 < G2_NK) WAIT_VM(24);
    else if (kt + 2 < G2_NK) WAIT_VM(16);
    else if (kt + 1 < G2_NK) WAIT_VM(8);
    else WAIT_VM(0);
    RAW_BARRIER();
    const char* sb = smem + (kt % G2_NS) * G2_STAGE;
#pragma unroll
    for (int ks = 0; ks < 4; ++ks) {
      bf16x8 hf[2], wf[2];
#pragma unroll
      for (int cc = 0; cc < 2; ++cc) hf[cc] = *(const bf16x8*)(sb + hrd[cc] + (((4 * ks + g) ^ r15) * 16));
#pragma unroll
      for (int ci = 0; ci < 2; ++ci)
        wf[ci] = cat8(tr_read(sb + wrd[ci][0] + ks * 4096), tr_read(sb + wrd[ci][1] + ks * 4096));
#pragma unroll
      for (int ci = 0; ci < 2; ++ci)
#pragma unroll
        for (int cc = 0; cc < 2; ++cc)
          acc[ci][cc] = __builtin_amdgcn_mfma_f32_16x16x32_bf16(wf[ci], hf[cc], acc[ci][cc], 0, 0, 0);
    }
    if (kt + G2_NS < G2_NK) {
      WAIT_LGKM0();
      RAW_BARRIER();
      stage(kt + G2_NS);
    }
  }
#pragma unroll
  for (int ci = 0; ci < 2; ++ci)
#pragma unroll
    for (int cc = 0; cc < 2; ++cc) {
      const int l = l0 + 16 * (2 * wl + cc) + (lane & 15);
      const int dd = d0 + 16 * (2 * wd + ci) + 4 * g;
      float o[4];
#pragma unroll
      for (int j = 0; j < 4; ++j) o[j] = bf2f(hres[ci][cc][j]) + rbf(acc[ci][cc][j]);
      *(u32x2*)(out + (size_t)l * D_ + dd) = u32x2{pack2(o[0], o[1]), pack2(o[2], o[3])};
    }
}

extern "C" void kernel_launch(void* const* d_in, const int* in_sizes, int n_in,
                              void* d_out, int out_size, void* d_ws, size_t ws_size,
                              hipStream_t stream) {
  const uint16_t* x = (const uint16_t*)d_in[0];
  const uint16_t* Are = (const uint16_t*)d_in[1];
  const uint16_t* Aim = (const uint16_t*)d_in[2];
  const uint16_t* Bre = (const uint16_t*)d_in[3];
  const uint16_t* Bim = (const uint16_t*)d_in[4];
  const uint16_t* Cre = (const uint16_t*)d_in[5];
  const uint16_t* Cim = (const uint16_t*)d_in[6];
  const uint16_t* logdt = (const uint16_t*)d_in[7];
  const uint16_t* Dskip = (const uint16_t*)d_in[8];
  const uint16_t* w1 = (const uint16_t*)d_in[10];
  const uint16_t* w2 = (const uint16_t*)d_in[11];
  char* ws = (char*)d_ws;
  hipFuncSetAttribute((const void*)k1_prep, hipFuncAttributeMaxDynamicSharedMemorySize, K1_LDS);
  hipFuncSetAttribute((const void*)k2_conv, hipFuncAttributeMaxDynamicSharedMemorySize, K2_LDS);
  hipFuncSetAttribute((const void*)k3_gemm1, hipFuncAttributeMaxDynamicSharedMemorySize, K3_LDS);
  hipFuncSetAttribute((const void*)k4_gemm2, hipFuncAttributeMaxDynamicSharedMemorySize, K4_LDS);
  k1_prep<<<K1_KG_BASE + D_, 256, K1_LDS, stream>>>(x, Are, Aim, Bre, Bim, Cre, Cim, logdt, w1, ws);
  k2_conv<<<D_ / 2, 256, K2_LDS, stream>>>(Dskip, ws);
  k3_gemm1<<<256, 256, K3_LDS, stream>>>(w1, ws);
  k4_gemm2<<<256, 256, K4_LDS, stream>>>(w2, ws, (uint16_t*)d_out);
}
```

```cpp
#include <hip/hip_runtime.h>
#include <hip/hip_bf16.h>
#include <stdint.h>

constexpr int L_ = 2048, D_ = 512, N_ = 128, F_ = 1792;

typedef __attribute__((ext_vector_type(8))) short bf16x8;
typedef __attribute__((ext_vector_type(4))) short bf16x4;
typedef __attribute__((ext_vector_type(8))) _Float16 f16x8;
typedef __attribute__((ext_vector_type(2))) _Float16 f16x2;
typedef __attribute__((ext_vector_type(4))) float f32x4;
typedef __attribute__((ext_vector_type(2))) float f32x2;
typedef __attribute__((ext_vector_type(4))) unsigned u32x4;
typedef __attribute__((ext_vector_type(2))) unsigned u32x2;
typedef __attribute__((address_space(3))) bf16x4 lds_bf16x4;
typedef __attribute__((address_space(3))) char lds_char;

__device__ __forceinline__ float bf2f(uint16_t v) { return __uint_as_float(((uint32_t)v) << 16); }
__device__ __forceinline__ float bflo(uint32_t v) { return __uint_as_float(v << 16); }
__device__ __forceinline__ float bfhi(uint32_t v) { return __uint_as_float(v & 0xffff0000u); }
__device__ __forceinline__ uint16_t f2bf(float f) {
  __hip_bfloat16 b = __float2bfloat16(f);
  return *reinterpret_cast<uint16_t*>(&b);
}
__device__ __forceinline__ float rbf(float f) { return bf2f(f2bf(f)); }
__device__ __forceinline__ uint32_t pack2(float lo, float hi) { return (uint32_t)f2bf(lo) | ((uint32_t)f2bf(hi) << 16); }
__device__ __forceinline__ uint32_t pack2h(float lo, float hi) {
  f16x2 v; v[0] = (_Float16)lo; v[1] = (_Float16)hi;
  return __builtin_bit_cast(uint32_t, v);
}

__device__ __forceinline__ void glds16(const void* g, unsigned lds_dst) {
  unsigned keep;
  asm volatile("s_mov_b32 %0, m0\n\ts_mov_b32 m0, %2\n\ts_nop 0\n\tglobal_load_lds_dwordx4 %1, off\n\ts_mov_b32 m0, %0"
               : "=&s"(keep) : "v"(g), "s"(lds_dst) : "memory");
}
#define WAIT_VM(n) asm volatile("s_waitcnt vmcnt(" #n ")" ::: "memory")
#define WAIT_LGKM0() asm volatile("s_waitcnt lgkmcnt(0)" ::: "memory")
#define RAW_BARRIER() do { asm volatile("" ::: "memory"); __builtin_amdgcn_s_barrier(); asm volatile("" ::: "memory"); } while (0)

constexpr size_t WS_UR = 0;
constexpr size_t WS_XT = WS_UR + (size_t)D_ * L_ * 2;
constexpr size_t WS_HT = WS_XT + (size_t)D_ * L_ * 2;
constexpr size_t WS_KG = WS_HT + (size_t)D_ * L_ * 2;
constexpr size_t WS_HR = WS_KG + (size_t)D_ * L_ * 2;
constexpr size_t WS_KS = WS_HR + (size_t)D_ * L_ * 2;
constexpr size_t WS_CS = WS_KS + 4096;
constexpr size_t WS_H1 = WS_CS + 8192;

constexpr int K1_LN_BLOCKS = 128;
constexpr int KS_ = 260;
constexpr int K1_LDS = 48 * KS_ * 4;

struct cplx { float r, i; };
__device__ __forceinline__ cplx cmul(cplx a, cplx b) {
  cplx o; o.r = a.r * b.r - a.i * b.i; o.i = a.r * b.i + a.i * b.r; return o;
}
__device__ __forceinline__ cplx cexp_(float x, float y) {
  float m = expf(x), s, c; sincosf(y, &s, &c);
  cplx o; o.r = m * c; o.i = m * s; return o;
}

__global__ __launch_bounds__(256, 3) void k1_prep(
    const uint16_t* __restrict__ x, const uint16_t* __restrict__ Are, const uint16_t* __restrict__ Aim,
    const uint16_t* __restrict__ Bre, const uint16_t* __restrict__ Bim, const uint16_t* __restrict__ Cre,
    const uint16_t* __restrict__ Cim, const uint16_t* __restrict__ logdt, char* __restrict__ ws) {
  extern __shared__ __attribute__((aligned(16))) char smem[];
  const int tid = threadIdx.x, lane = tid & 63, wv = tid >> 6;
  const int bid = blockIdx.x;
  if (bid >= K1_LN_BLOCKS) {
    const int d = bid - K1_LN_BLOCKS;
    float* Btab = (float*)smem;
    float* Atab = Btab + 16 * KS_;
    const int n = tid & 127, part = tid >> 7;
    const float dt = rbf(expf(bf2f(logdt[d])));
    const float zx = dt * bf2f(Are[d * N_ + n]);
    const float zy = dt * bf2f(Aim[d * N_ + n]);
    cplx c;
    {
      float br = dt * bf2f(Bre[d * N_ + n]), bi = dt * bf2f(Bim[d * N_ + n]);
      float cr = bf2f(Cre[d * N_ + n]), ci = bf2f(Cim[d * N_ + n]);
      c.r = cr * br - ci * bi; c.i = cr * bi + ci * br;
    }
    auto putA = [&](int row, cplx v) { *(f32x2*)(Atab + row * KS_ + 2 * n) = f32x2{v.r, -v.i}; };
    auto putB = [&](int row, cplx v) { *(f32x2*)(Btab + row * KS_ + 2 * n) = f32x2{v.r, v.i}; };
    const cplx g1 = cexp_(128.f * zx, 128.f * zy);
    cplx late[9];
    if (part == 0) {
      const cplx e1 = cexp_(zx, zy);
      const cplx e8 = cexp_(8.f * zx, 8.f * zy);
      cplx lo; lo.r = 1.f; lo.i = 0.f;
      putB(0, lo);
      lo = e8; putB(1, lo);
#pragma unroll
      for (int b = 2; b < 16; ++b) { lo = cmul(lo, e8); putB(b, lo); }
      cplx ce = c;
#pragma unroll
      for (int e = 0; e < 8; ++e) {
        putA(e, ce);
        putA(8 + e, cmul(ce, g1));
        if (e < 7) ce = cmul(ce, e1);
      }
#pragma unroll
      for (int a = 0; a < 9; ++a) { late[a].r = 0.f; late[a].i = 0.f; }
    } else {
      const cplx e2 = cexp_(2.f * zx, 2.f * zy);
      const cplx c2 = cmul(c, e2), c4 = cmul(c2, e2), c6 = cmul(c4, e2);
      cplx g = cmul(g1, g1);
#pragma unroll
      for (int a = 2; a < 4; ++a) {
        putA(16 + 4 * (a - 2) + 0, cmul(c, g));
        putA(16 + 4 * (a - 2) + 1, cmul(c2, g));
        putA(16 + 4 * (a - 2) + 2, cmul(c4, g));
        putA(16 + 4 * (a - 2) + 3, cmul(c6, g));
        g = cmul(g, g1);
      }
#pragma unroll
      for (int a = 4; a < 8; ++a) {
        putA(24 + 2 * (a - 4) + 0, cmul(c, g));
        putA(24 + 2 * (a - 4) + 1, cmul(c4, g));
        g = cmul(g, g1);
      }
#pragma unroll
      for (int a = 8; a < 17; ++a) {
        late[a - 8] = cmul(c, g);
        if (a < 16) g = cmul(g, g1);
      }
    }
    __syncthreads();
    f32x4 acc[3] = {f32x4{0, 0, 0, 0}, f32x4{0, 0, 0, 0}, f32x4{0, 0, 0, 0}};
    const int r16 = lane & 15, kk = lane >> 4;
    f32x4 bv[4];
#pragma unroll
    for (int gi = 0; gi < 4; ++gi) {
      const int k0 = (wv * 4 + gi) * 16 + 4 * kk;
      bv[gi] = *(const f32x4*)(Btab + r16 * KS_ + k0);
      f32x4 av[2];
#pragma unroll
      for (int r = 0; r < 2; ++r) av[r] = *(const f32x4*)(Atab + (16 * r + r16) * KS_ + k0);
#pragma unroll
      for (int t = 0; t < 4; ++t)
#pragma unroll
        for (int r = 0; r < 2; ++r)
          acc[r] = __builtin_amdgcn_mfma_f32_16x16x4f32(av[r][t], bv[gi][t], acc[r], 0, 0, 0);
    }
    __syncthreads();
    if (part == 1) {
#pragma unroll
      for (int a = 0; a < 9; ++a) putA(a, late[a]);
    }
    __syncthreads();
#pragma unroll
    for (int gi = 0; gi < 4; ++gi) {
      const int k0 = (wv * 4 + gi) * 16 + 4 * kk;
      const f32x4 av = *(const f32x4*)(Atab + r16 * KS_ + k0);
#pragma unroll
      for (int t = 0; t < 4; ++t) acc[2] = __builtin_amdgcn_mfma_f32_16x16x4f32(av[t], bv[gi][t], acc[2], 0, 0, 0);
    }
    float* Kv = Atab + 16 * KS_;
    {
      const int col = lane & 15, rq = lane >> 4;
#pragma unroll
      for (int r = 0; r < 3; ++r)
#pragma unroll
        for (int j = 0; j < 4; ++j) Kv[wv * 768 + (16 * r + 4 * rq + j) * 16 + col] = acc[r][j];
    }
    __syncthreads();
    float* Kf = Btab;
    {
      float mx = 0.f;
#pragma unroll
      for (int i = 0; i < 3; ++i) {
        const int ix = tid + 256 * i;
        const float v = (Kv[ix] + Kv[768 + ix]) + (Kv[1536 + ix] + Kv[2304 + ix]);
        Kf[ix] = v;
        if (ix <= 640) mx = fmaxf(mx, fabsf(v));
      }
#pragma unroll
      for (int o = 32; o > 0; o >>= 1) mx = fmaxf(mx, __shfl_xor(mx, o));
      if (lane == 0) Kf[768 + wv] = mx;
    }
    __syncthreads();
    {
      const float mx = fmaxf(fmaxf(Kf[768], Kf[769]), fmaxf(Kf[770], Kf[771]));
      int ex = (int)((__float_as_uint(mx) >> 23) & 255u) - 127;
      int sh = 14 - ex;
      sh = sh > 126 ? 126 : (sh < -126 ? -126 : sh);
      const float scale = __uint_as_float((uint32_t)(127 + sh) << 23);
      if (tid == 0) ((float*)(ws + WS_KS))[d] = __uint_as_float((uint32_t)(127 - sh) << 23);
      uint32_t o[4];
#pragma unroll
      for (int j2 = 0; j2 < 4; ++j2) {
        float kv[2];
#pragma unroll
        for (int s = 0; s < 2; ++s) {
          const int tt = 8 * tid + 2 * j2 + s;
          const int lb = (int)rbf((float)tt);
          const int b = (lb >> 3) & 15;
          int row;
          if (lb < 256) row = ((lb >> 7) << 3) + (lb & 7);
          else if (lb < 512) row = 16 + (((lb - 256) >> 7) << 2) + ((lb & 7) >> 1);
          else if (lb < 1024) row = 24 + (((lb - 512) >> 7) << 1) + ((lb & 7) >> 2);
          else row = 32 + ((lb - 1024) >> 7);
          kv[s] = Kf[row * 16 + b] * scale;
        }
        o[j2] = pack2h(kv[0], kv[1]);
      }
      *(u32x4*)((uint16_t*)(ws + WS_KG) + (size_t)d * L_ + 8 * tid) = u32x4{o[0], o[1], o[2], o[3]};
    }
  } else {
    constexpr int TS = 1024 + 16;
    const int l0 = bid * 16;
    u32x4 xr[4];
#pragma unroll
    for (int i = 0; i < 4; ++i) xr[i] = *(const u32x4*)(x + (size_t)(l0 + wv * 4 + i) * D_ + lane * 8);
#pragma unroll
    for (int i = 0; i < 4; ++i) {
      float v[8];
#pragma unroll
      for (int j = 0; j < 4; ++j) { v[2 * j] = bflo(xr[i][j]); v[2 * j + 1] = bfhi(xr[i][j]); }
      float s = 0.f;
#pragma unroll
      for (int j = 0; j < 8; ++j) s += v[j];
#pragma unroll
      for (int o = 32; o > 0; o >>= 1) s += __shfl_xor(s, o);
      const float mean = s * (1.0f / D_);
      float q = 0.f;
#pragma unroll
      for (int j = 0; j < 8; ++j) { float dd = v[j] - mean; q += dd * dd; }
#pragma unroll
      for (int o = 32; o > 0; o >>= 1) q += __shfl_xor(q, o);
      const float m = rbf(mean), vb = rbf(q * (1.0f / D_));
      const float sd = rbf(sqrtf(rbf(vb + 1e-5f)));
      u32x4 o;
#pragma unroll
      for (int j = 0; j < 4; ++j) o[j] = pack2h(rbf(rbf(v[2 * j] - m) / sd), rbf(rbf(v[2 * j + 1] - m) / sd));
      *(u32x4*)(smem + (wv * 4 + i) * TS + lane * 16) = o;
      *(u32x4*)(smem + (16 + wv * 4 + i) * TS + lane * 16) = xr[i];
    }
    __syncthreads();
#pragma unroll
    for (int pass = 0; pass < 2; ++pass) {
      uint32_t v[16];
#pragma unroll
      for (int r = 0; r < 16; ++r) v[r] = *(const uint32_t*)(smem + (pass * 16 + (pass == 0 ? 15 - r : r)) * TS + tid * 4);
      uint16_t* dst = (uint16_t*)(ws + (pass == 0 ? WS_UR : WS_XT));
      u32x4 a0, a1, b0, b1;
#pragma unroll
      for (int j = 0; j < 4; ++j) {
        a0[j] = (v[2 * j] & 0xffffu) | (v[2 * j + 1] << 16);
        b0[j] = (v[2 * j] >> 16) | (v[2 * j + 1] & 0xffff0000u);
        a1[j] = (v[8 + 2 * j] & 0xffffu) | (v[8 + 2 * j + 1] << 16);
        b1[j] = (v[8 + 2 * j] >> 16) | (v[8 + 2 * j + 1] & 0xffff0000u);
      }
      uint16_t* p0 = dst + (size_t)(2 * tid) * L_ + (pass == 0 ? (L_ - 16 - l0) : l0);
      *(u32x4*)(p0) = a0; *(u32x4*)(p0 + 8) = a1;
      *(u32x4*)(p0 + L_) = b0; *(u32x4*)(p0 + L_ + 8) = b1;
    }
  }
}

constexpr int UC_S = 4160;
constexpr int UC_B4 = 4 * UC_S + 32;
constexpr int UREG = UC_B4 + 4 * UC_S;
constexpr int KOFF = UREG;
constexpr int K2_LDS = KOFF + 512 + 4096;
constexpr int K2_CS_BLOCKS = 28;

__global__ __launch_bounds__(256, 4) void k2_conv(const uint16_t* __restrict__ Dskip, const uint16_t* __restrict__ w1,
                                                  char* __restrict__ ws) {
  extern __shared__ __attribute__((aligned(16))) char smem[];
  const int tid = threadIdx.x, lane = tid & 63;
  const int wv = __builtin_amdgcn_readfirstlane(tid >> 6);
  const int bid = blockIdx.x;
  if (bid < K2_CS_BLOCKS) {
    const int cb = bid;
    const int cp = tid & 31, rg = tid >> 5;
    float s0 = 0.f, s1 = 0.f;
#pragma unroll 16
    for (int r = 0; r < 64; ++r) {
      uint32_t v = *(const uint32_t*)(w1 + (size_t)(rg * 64 + r) * F_ + cb * 64 + cp * 2);
      s0 += bflo(v); s1 += bfhi(v);
    }
    float* red = (float*)smem;
    red[rg * 64 + cp * 2] = s0; red[rg * 64 + cp * 2 + 1] = s1;
    __syncthreads();
    if (tid < 64) {
      float s = 0.f;
#pragma unroll
      for (int g = 0; g < 8; ++g) s += red[g * 64 + tid];
      ((float*)(ws + WS_CS))[cb * 64 + tid] = s;
    }
    return;
  }
  const int d = bid - K2_CS_BLOCKS;
  const unsigned lbase = (unsigned)(size_t)(lds_char*)smem;
  const uint16_t* uR = (const uint16_t*)(ws + WS_UR) + (size_t)d * L_;
  const uint16_t* xT = (const uint16_t*)(ws + WS_XT) + (size_t)d * L_;
  const uint16_t* Kg = (const uint16_t*)(ws + WS_KG) + (size_t)d * L_;
  const u32x4 ua = *(const u32x4*)(uR + tid * 8);
  u32x4 ub = *(const u32x4*)(uR + (tid < 255 ? tid + 1 : tid) * 8);
  const u32x4 xe = *(const u32x4*)(xT + tid * 8);
  const u32x4 ue = *(const u32x4*)(uR + (255 - tid) * 8);
  const float dsk = bf2f(Dskip[d]);
  const float kinv = ((const float*)(ws + WS_KS))[d];
  glds16(Kg + wv * 512 + lane * 8, lbase + KOFF + 512 + wv * 1024);
  if (tid < 32) *(u32x4*)(smem + KOFF + tid * 16) = u32x4{0, 0, 0, 0};
  else if (tid < 40) {
    const int j = tid - 32;
    *(u32x4*)(smem + (j & 3) * UC_S + (j >> 2) * UC_B4 + 256 * 16) = u32x4{0, 0, 0, 0};
  }
  if (tid == 255) ub = u32x4{0, 0, 0, 0};
  {
    const uint32_t S[8] = {ua[0], ua[1], ua[2], ua[3], ub[0], ub[1], ub[2], ub[3]};
#pragma unroll
    for (int ml = 0; ml < 8; ++ml) {
      const int s = 7 - ml;
      u32x4 o;
#pragma unroll
      for (int i = 0; i < 4; ++i)
        o[i] = (s & 1) ? __builtin_amdgcn_alignbit(S[(s >> 1) + i + 1], S[(s >> 1) + i], 16) : S[(s >> 1) + i];
      *(u32x4*)(smem + (ml & 3) * UC_S + (ml >> 2) * UC_B4 + tid * 16) = o;
    }
  }
  WAIT_VM(0);
  __syncthreads();

  const int m = lane & 15, kg = lane >> 4;
  const int ml = m & 7, mh = m >> 3;
  const char* abase = smem + (ml & 3) * UC_S + (ml >> 2) * UC_B4 + 16 * (253 - mh + kg) - 128 * wv;
  const char* bbase = smem + KOFF + 512 + 32 * m + 16 * kg - 32 - 128 * wv;
  f32x4 acc[8];
#pragma unroll
  for (int t = 0; t < 8; ++t) acc[t] = f32x4{0, 0, 0, 0};
#pragma unroll
  for (int s = 0; s < 2; ++s) {
    f16x8 af[8], bfr[8];
#pragma unroll
    for (int j = 0; j < 8; ++j) {
      af[j] = *(const f16x8*)(abase - 512 * j - 64 * s);
      bfr[j] = *(const f16x8*)(bbase + 512 * j - 64 * s);
    }
#pragma unroll
    for (int T = 0; T < 8; ++T)
#pragma unroll
      for (int al = 0; al <= T; ++al)
        acc[T] = __builtin_amdgcn_mfma_f32_16x16x32_f16(af[al], bfr[T - al], acc[T], 0, 0, 0);
  }
  __syncthreads();
#pragma unroll
  for (int T = 0; T < 8; ++T)
    *(f32x4*)(smem + wv * 8192 + T * 1024 + m * 64 + kg * 16) = acc[T];
  __syncthreads();
  {
    uint32_t out[4];
#pragma unroll
    for (int i = 0; i < 2; ++i) {
      const char* rb = smem + tid * 32 + i * 16;
      const f32x4 s0 = *(const f32x4*)(rb), s1 = *(const f32x4*)(rb + 8192);
      const f32x4 s2 = *(const f32x4*)(rb + 16384), s3 = *(const f32x4*)(rb + 24576);
#pragma unroll
      for (int j2 = 0; j2 < 2; ++j2) {
        const int w = 2 * i + j2;
        const uint32_t xv = xe[w];
        const uint32_t uw = ue[3 - w];
        const f16x2 uv = __builtin_bit_cast(f16x2, uw);
        const float y0 = rbf(((s0[2 * j2] + s1[2 * j2]) + (s2[2 * j2] + s3[2 * j2])) * kinv);
        const float y1 = rbf(((s0[2 * j2 + 1] + s1[2 * j2 + 1]) + (s2[2 * j2 + 1] + s3[2 * j2 + 1])) * kinv);
        const float r0 = rbf(y0 + rbf((float)uv[1] * dsk));
        const float r1 = rbf(y1 + rbf((float)uv[0] * dsk));
        out[w] = pack2(bflo(xv) + r0, bfhi(xv) + r1);
      }
    }
    *(u32x4*)((uint16_t*)(ws + WS_HT) + (size_t)d * L_ + tid * 8) = u32x4{out[0], out[1], out[2], out[3]};
  }
}

constexpr int G1_HS = 64 * 256;
constexpr int G1_WS = 64 * 224;
constexpr int G1_STAGE = G1_HS + G1_WS;
constexpr int G1_NS = 5;
constexpr int K3_LDS = G1_NS * G1_STAGE;

__device__ __forceinline__ bf16x4 tr_read(const char* p) {
  return __builtin_amdgcn_ds_read_tr16_b64_v4i16((lds_bf16x4*)p);
}
__device__ __forceinline__ bf16x8 cat8(bf16x4 a, bf16x4 b) {
  return bf16x8{a[0], a[1], a[2], a[3], b[0], b[1], b[2], b[3]};
}
__device__ __forceinline__ void swap16(uint32_t& a, uint32_t& b) {
  auto r = __builtin_amdgcn_permlane16_swap(a, b, false, false);
  a = r[0]; b = r[1];
}

__global__ __launch_bounds__(256) void k3_gemm1(const uint16_t* __restrict__ w1, char* __restrict__ ws) {
  extern __shared__ __attribute__((aligned(16))) char smem[];
  const int tid = threadIdx.x, lane = tid & 63;
  const int wv = __builtin_amdgcn_readfirstlane(tid >> 6);
  const int bid = blockIdx.x;
  const int xcd = bid & 7, idx = bid >> 3;
  const int lt = 4 * (xcd >> 1) + (idx & 3), ft = 8 * (xcd & 1) + (idx >> 2);
  const int l0 = lt * 128, f0 = ft * 112;
  const unsigned lbase = (unsigned)(size_t)(lds_char*)smem;
  const uint16_t* hT = (const uint16_t*)(ws + WS_HT);
  const int g = lane >> 4, q = (lane >> 2) & 3, pp = lane & 3;
  int hoff[4];
#pragma unroll
  for (int i = 0; i < 4; ++i) {
    const int row = (wv * 4 + i) * 4 + (lane >> 4);
    hoff[i] = row * L_ + l0 + 8 * ((lane & 15) ^ (2 * (row & 7)));
  }
  const int nw = (wv < 2) ? 4 : 3;
  const int wi0 = (wv < 2) ? wv * 4 : 8 + (wv - 2) * 3;
  int woff[4];
#pragma unroll
  for (int i = 0; i < 4; ++i) {
    const int nn = (wi0 + (i < nw ? i : 0)) * 64 + lane;
    const int row = nn / 14, ch = nn - row * 14;
    woff[i] = row * F_ + f0 + 8 * ch;
  }
  const float* cs = (const float*)(ws + WS_CS);
  f32x4 c4[7];
#pragma unroll
  for (int ci = 0; ci < 7; ++ci) c4[ci] = *(const f32x4*)(cs + f0 + 16 * ci + 4 * g);
  auto stage = [&](int kt) {
    const unsigned sb = lbase + (kt % G1_NS) * G1_STAGE;
    const uint16_t* hs = hT + (size_t)kt * 64 * L_;
    const uint16_t* wsrc = w1 + (size_t)kt * 64 * F_;
#pragma unroll
    for (int i = 0; i < 4; ++i) glds16(hs + hoff[i], sb + (wv * 4 + i) * 1024);
#pragma unroll
    for (int i = 0; i < 3; ++i) glds16(wsrc + woff[i], sb + G1_HS + (wi0 + i) * 1024);
    if (wv < 2) glds16(wsrc + woff[3], sb + G1_HS + (wi0 + 3) * 1024);
  };
#pragma unroll
  for (int s = 0; s < G1_NS; ++s) stage(s);

  const int xv = 2 * (4 * (g & 1) + q);
  int hrd[2];
#pragma unroll
  for (int cc = 0; cc < 2; ++cc)
    hrd[cc] = (4 * g + q) * 256 + ((((2 * (2 * wv + cc)) ^ xv) + (pp >> 1)) * 16) + 8 * (pp & 1);
  const int wrd = G1_HS + (4 * g + q) * 224 + 8 * pp;

  f32x4 acc[7][2], accS[2], accG[2];
#pragma unroll
  for (int i = 0; i < 7; ++i) { acc[i][0] = f32x4{0, 0, 0, 0}; acc[i][1] = f32x4{0, 0, 0, 0}; }
  accS[0] = accS[1] = accG[0] = accG[1] = f32x4{0, 0, 0, 0};
  const bf16x8 ones = {0x3f80, 0x3f80, 0x3f80, 0x3f80, 0x3f80, 0x3f80, 0x3f80, 0x3f80};
  bf16x8 hsave[2];

  struct Frag { bf16x8 h[2], w[7]; };
  auto ldfrag = [&](Frag& F, int kt, int ks) {
    const char* sb = smem + (kt % G1_NS) * G1_STAGE;
#pragma unroll
    for (int cc = 0; cc < 2; ++cc)
      F.h[cc] = cat8(tr_read(sb + hrd[cc] + ks * 8192), tr_read(sb + hrd[cc] + ks * 8192 + 4096));
#pragma unroll
    for (int ci = 0; ci < 7; ++ci)
      F.w[ci] = cat8(tr_read(sb + wrd + ks * 32 * 224 + 32 * ci), tr_read(sb + wrd + (ks * 32 + 16) * 224 + 32 * ci));
  };
  auto mma = [&](const Frag& F, int kstep) {
#pragma unroll
    for (int ci = 0; ci < 7; ++ci)
#pragma unroll
      for (int cc = 0; cc < 2; ++cc)
        acc[ci][cc] = __builtin_amdgcn_mfma_f32_16x16x32_bf16(F.w[ci], F.h[cc], acc[ci][cc], 0, 0, 0);
#pragma unroll
    for (int cc = 0; cc < 2; ++cc) {
      accS[cc] = __builtin_amdgcn_mfma_f32_16x16x32_bf16(ones, F.h[cc], accS[cc], 0, 0, 0);
      accG[cc] = __builtin_amdgcn_mfma_f32_16x16x32_bf16(F.h[cc], F.h[cc], accG[cc], 0, 0, 0);
    }
    if (kstep == ft) { hsave[0] = F.h[0]; hsave[1] = F.h[1]; }
  };
#define K3_WAIT(r) do { if (wv < 2) { WAIT_VM_N(8 * (r)); } else { WAIT_VM_N(7 * (r)); } } while (0)
#define WAIT_VM_N(n) asm volatile("s_waitcnt vmcnt(%0)" :: "i"(n) : "memory")
  Frag F0, F1;
  K3_WAIT(4);
  RAW_BARRIER();
  ldfrag(F0, 0, 0);
#pragma unroll
  for (int kt = 0; kt < 8; ++kt) {
    ldfrag(F1, kt, 1);
    mma(F0, 2 * kt);
    if (kt < 7) {
      if (kt == 0) K3_WAIT(3);
      else if (kt < 5) K3_WAIT(2);
      else if (kt == 5) K3_WAIT(1);
      else K3_WAIT(0);
      RAW_BARRIER();
      if (kt >= 1 && kt + 4 < 8) stage(kt + 4);
      ldfrag(F0, kt + 1, 0);
    }
    mma(F1, 2 * kt + 1);
  }
  uint16_t* H1 = (uint16_t*)(ws + WS_H1);
  uint32_t pk[7][2][2];
  const int r15 = lane & 15;
#pragma unroll
  for (int cc = 0; cc < 2; ++cc) {
    const float s = accS[cc][0];
    float dg = (pp == 0) ? accG[cc][0] : (pp == 1) ? accG[cc][1] : (pp == 2) ? accG[cc][2] : accG[cc][3];
    const float sq = __shfl(dg, r15 + 16 * (r15 >> 2));
    const float mean = s * (1.0f / D_);
    const float var = fmaxf(sq * (1.0f / D_) - mean * mean, 0.f);
    const float mm = rbf(mean);
    const float sd = rbf(sqrtf(rbf(rbf(var) + 1e-5f)));
    const float rs = 1.0f / sd;
#pragma unroll
    for (int ci = 0; ci < 7; ++ci) {
      float o[4];
#pragma unroll
      for (int j = 0; j < 4; ++j) {
        const float v = rbf((acc[ci][cc][j] - mm * c4[ci][j]) * rs);
        o[j] = v * __builtin_amdgcn_rcpf(1.0f + __expf(-v));
      }
      pk[ci][cc][0] = pack2(o[0], o[1]); pk[ci][cc][1] = pack2(o[2], o[3]);
    }
  }
  const int lrow = l0 + 32 * wv + r15;
#pragma unroll
  for (int cc = 0; cc < 2; ++cc)
#pragma unroll
    for (int cp = 0; cp < 3; ++cp) {
      swap16(pk[2 * cp][cc][0], pk[2 * cp + 1][cc][0]);
      swap16(pk[2 * cp][cc][1], pk[2 * cp + 1][cc][1]);
      uint16_t* p = H1 + (size_t)(lrow + 16 * cc) * F_ + f0 + 16 * (2 * cp + (g & 1)) + 8 * (g >> 1);
      *(u32x4*)p = u32x4{pk[2 * cp][cc][0], pk[2 * cp][cc][1], pk[2 * cp + 1][cc][0], pk[2 * cp + 1][cc][1]};
    }
  {
    swap16(pk[6][0][0], pk[6][1][0]);
    swap16(pk[6][0][1], pk[6][1][1]);
    uint16_t* p = H1 + (size_t)(lrow + 16 * (g & 1)) * F_ + f0 + 96 + 8 * (g >> 1);
    *(u32x4*)p = u32x4{pk[6][0][0], pk[6][0][1], pk[6][1][0], pk[6][1][1]};
  }
#pragma unroll
  for (int cc = 0; cc < 2; ++cc) {
    u32x4 hv = __builtin_bit_cast(u32x4, hsave[cc]);
    uint32_t a0 = hv[0], a1 = hv[1], b0 = hv[2], b1 = hv[3];
    swap16(a0, b0); swap16(a1, b1);
    uint16_t* p = (uint16_t*)(ws + WS_HR) + (size_t)(lrow + 16 * cc) * D_ + 32 * ft + 16 * (g & 1) + 8 * (g >> 1);
    *(u32x4*)p = u32x4{a0, a1, b0, b1};
  }
}

constexpr int G2_HS = 64 * 256;
constexpr int G2_WS = 128 * 128;
constexpr int G2_STAGE = G2_HS + G2_WS;
constexpr int G2_NS = 5;
constexpr int G2_NK = F_ / 128;
constexpr int K4_LDS = G2_NS * G2_STAGE;

__global__ __launch_bounds__(256) void k4_gemm2(const uint16_t* __restrict__ w2, char* __restrict__ ws,
                                                uint16_t* __restrict__ out) {
  extern __shared__ __attribute__((aligned(16))) char smem[];
  const int tid = threadIdx.x, lane = tid & 63;
  const int wv = __builtin_amdgcn_readfirstlane(tid >> 6);
  const int bid = blockIdx.x;
  const int xcd = bid & 7, idx = bid >> 3;
  const int lt = 4 * xcd + (idx & 3), dtile = idx >> 2;
  const int l0 = lt * 64, d0 = dtile * 64;
  const unsigned lbase = (unsigned)(size_t)(lds_char*)smem;
  const uint16_t* H1 = (const uint16_t*)(ws + WS_H1);
  const int g = lane >> 4, q = (lane >> 2) & 3, pp = lane & 3;
  const int wd = wv & 1, wl = wv >> 1;
  const int r15 = lane & 15;
  int hoff[4], woff[4];
#pragma unroll
  for (int i = 0; i < 4; ++i) {
    const int row = (wv * 4 + i) * 4 + (lane >> 4);
    hoff[i] = (l0 + row) * F_ + 8 * ((lane & 15) ^ (row & 15));
    const int r2 = (wv * 4 + i) * 8 + (lane >> 3);
    const int gf = ((r2 >> 1) & 1) | (((r2 >> 3) & 1) << 1);
    woff[i] = r2 * D_ + d0 + 8 * ((lane & 7) ^ (2 * gf));
  }
  const uint16_t* hR = (const uint16_t*)(ws + WS_HR);
  u32x4 hres[2];
#pragma unroll
  for (int cc = 0; cc < 2; ++cc)
    hres[cc] = *(const u32x4*)(hR + (size_t)(l0 + 16 * (2 * wl + cc) + r15) * D_ + d0 + 32 * wd + 16 * (g & 1) + 8 * (g >> 1));
  auto stage = [&](int kt) {
    const unsigned sb = lbase + (kt % G2_NS) * G2_STAGE;
#pragma unroll
    for (int i = 0; i < 4; ++i) glds16(w2 + woff[i] + (size_t)kt * 128 * D_, sb + G2_HS + (wv * 4 + i) * 1024);
#pragma unroll
    for (int i = 0; i < 4; ++i) glds16(H1 + hoff[i] + kt * 128, sb + (wv * 4 + i) * 1024);
  };
#pragma unroll
  for (int s = 0; s < G2_NS; ++s) stage(s);

  int hrd[2];
#pragma unroll
  for (int cc = 0; cc < 2; ++cc) hrd[cc] = (16 * (2 * wl + cc) + r15) * 256;
  int wrd[2][2];
#pragma unroll
  for (int ci = 0; ci < 2; ++ci)
#pragma unroll
    for (int t = 0; t < 2; ++t) {
      const int row = 8 * g + 4 * t + q;
      const int gf = ((row >> 1) & 1) | (((row >> 3) & 1) << 1);
      wrd[ci][t] = G2_HS + row * 128 + (((2 * (2 * wd + ci)) ^ (2 * gf)) + (pp >> 1)) * 16 + 8 * (pp & 1);
    }
  f32x4 acc[2][2];
#pragma unroll
  for (int i = 0; i < 2; ++i) { acc[i][0] = f32x4{0, 0, 0, 0}; acc[i][1] = f32x4{0, 0, 0, 0}; }

  struct Frag { bf16x8 h[2][2], w[2][2]; };
  auto ldfrag = [&](Frag& F, int kt, int kh) {
    const char* sb = smem + (kt % G2_NS) * G2_STAGE;
#pragma unroll
    for (int k2 = 0; k2 < 2; ++k2) {
      const int ks = 2 * kh + k2;
#pragma unroll
      for (int cc = 0; cc < 2; ++cc) F.h[k2][cc] = *(const bf16x8*)(sb + hrd[cc] + (((4 * ks + g) ^ r15) * 16));
#pragma unroll
      for (int ci = 0; ci < 2; ++ci)
        F.w[k2][ci] = cat8(tr_read(sb + wrd[ci][0] + ks * 4096), tr_read(sb + wrd[ci][1] + ks * 4096));
    }
  };
  auto mma = [&](const Frag& F) {
#pragma unroll
    for (int k2 = 0; k2 < 2; ++k2)
#pragma unroll
      for (int ci = 0; ci < 2; ++ci)
#pragma unroll
        for (int cc = 0; cc < 2; ++cc)
          acc[ci][cc] = __builtin_amdgcn_mfma_f32_16x16x32_bf16(F.w[k2][ci], F.h[k2][cc], acc[ci][cc], 0, 0, 0);
  };
  Frag F0, F1;
  WAIT_VM(32);
  RAW_BARRIER();
  ldfrag(F0, 0, 0);
#pragma unroll
  for (int kt = 0; kt < G2_NK; ++kt) {
    ldfrag(F1, kt, 1);
    mma(F0);
    if (kt < G2_NK - 1) {
      if (kt == 0) WAIT_VM(24);
      else if (kt < G2_NK - 3) WAIT_VM(16);
      else if (kt == G2_NK - 3) WAIT_VM(8);
      else WAIT_VM(0);
      RAW_BARRIER();
      if (kt >= 1 && kt + 4 < G2_NK) stage(kt + 4);
      ldfrag(F0, kt + 1, 0);
    }
    mma(F1);
  }
#pragma unroll
  for (int cc = 0; cc < 2; ++cc) {
    const size_t o = (size_t)(l0 + 16 * (2 * wl + cc) + r15) * D_ + d0 + 32 * wd + 16 * (g & 1) + 8 * (g >> 1);
    const u32x4 hv = hres[cc];
    uint32_t a0 = pack2(acc[0][cc][0], acc[0][cc][1]), a1 = pack2(acc[0][cc][2], acc[0][cc][3]);
    uint32_t b0 = pack2(acc[1][cc][0], acc[1][cc][1]), b1 = pack2(acc[1][cc][2], acc[1][cc][3]);
    swap16(a0, b0); swap16(a1, b1);
    const uint32_t v[4] = {a0, a1, b0, b1};
    u32x4 r;
#pragma unroll
    for (int j = 0; j < 4; ++j) r[j] = pack2(bflo(hv[j]) + bflo(v[j]), bfhi(hv[j]) + bfhi(v[j]));
    *(u32x4*)(out + o) = r;
  }
}

extern "C" void kernel_launch(void* const* d_in, const int* in_sizes, int n_in,
                              void* d_out, int out_size, void* d_ws, size_t ws_size,
                              hipStream_t stream) {
  const uint16_t* x = (const uint16_t*)d_in[0];
  const uint16_t* Are = (const uint16_t*)d_in[1];
  const uint16_t* Aim = (const uint16_t*)d_in[2];
  const uint16_t* Bre = (const uint16_t*)d_in[3];
  const uint16_t* Bim = (const uint16_t*)d_in[4];
  const uint16_t* Cre = (const uint16_t*)d_in[5];
  const uint16_t* Cim = (const uint16_t*)d_in[6];
  const uint16_t* logdt = (const uint16_t*)d_in[7];
  const uint16_t* Dskip = (const uint16_t*)d_in[8];
  const uint16_t* w1 = (const uint16_t*)d_in[10];
  const uint16_t* w2 = (const uint16_t*)d_in[11];
  char* ws = (char*)d_ws;
  (void)hipFuncSetAttribute((const void*)k1_prep, hipFuncAttributeMaxDynamicSharedMemorySize, K1_LDS);
  (void)hipFuncSetAttribute((const void*)k2_conv, hipFuncAttributeMaxDynamicSharedMemorySize, K2_LDS);
  (void)hipFuncSetAttribute((const void*)k3_gemm1, hipFuncAttributeMaxDynamicSharedMemorySize, K3_LDS);
  (void)hipFuncSetAttribute((const void*)k4_gemm2, hipFuncAttributeMaxDynamicSharedMemorySize, K4_LDS);
  k1_prep<<<K1_LN_BLOCKS + D_, 256, K1_LDS, stream>>>(x, Are, Aim, Bre, Bim, Cre, Cim, logdt, ws);
  k2_conv<<<K2_CS_BLOCKS + D_, 256, K2_LDS, stream>>>(Dskip, w1, ws);
  k3_gemm1<<<256, 256, K3_LDS, stream>>>(w1, ws);
  k4_gemm2<<<256, 256, K4_LDS, stream>>>(w2, ws, (uint16_t*)d_out);
}
```

```cpp
#include <hip/hip_runtime.h>
#include <hip/hip_bf16.h>
#include <stdint.h>

constexpr int L_ = 2048, D_ = 512, N_ = 128, F_ = 1792;

typedef __attribute__((ext_vector_type(8))) short bf16x8;
typedef __attribute__((ext_vector_type(4))) short bf16x4;
typedef __attribute__((ext_vector_type(8))) _Float16 f16x8;
typedef __attribute__((ext_vector_type(2))) _Float16 f16x2;
typedef __attribute__((ext_vector_type(4))) float f32x4;
typedef __attribute__((ext_vector_type(2))) float f32x2;
typedef __attribute__((ext_vector_type(4))) unsigned u32x4;
typedef __attribute__((ext_vector_type(2))) unsigned u32x2;
typedef __attribute__((address_space(3))) bf16x4 lds_bf16x4;
typedef __attribute__((address_space(3))) char lds_char;

__device__ __forceinline__ float bf2f(uint16_t v) { return __uint_as_float(((uint32_t)v) << 16); }
__device__ __forceinline__ float bflo(uint32_t v) { return __uint_as_float(v << 16); }
__device__ __forceinline__ float bfhi(uint32_t v) { return __uint_as_float(v & 0xffff0000u); }
__device__ __forceinline__ uint16_t f2bf(float f) {
  __hip_bfloat16 b = __float2bfloat16(f);
  return *reinterpret_cast<uint16_t*>(&b);
}
__device__ __forceinline__ float rbf(float f) { return bf2f(f2bf(f)); }
__device__ __forceinline__ uint32_t pack2(float lo, float hi) { return (uint32_t)f2bf(lo) | ((uint32_t)f2bf(hi) << 16); }
__device__ __forceinline__ uint32_t pack2h(float lo, float hi) {
  f16x2 v; v[0] = (_Float16)lo; v[1] = (_Float16)hi;
  return __builtin_bit_cast(uint32_t, v);
}

__device__ __forceinline__ void glds16(const void* g, unsigned lds_dst) {
  unsigned keep;
  asm volatile("s_mov_b32 %0, m0\n\ts_mov_b32 m0, %2\n\ts_nop 0\n\tglobal_load_lds_dwordx4 %1, off\n\ts_mov_b32 m0, %0"
               : "=&s"(keep) : "v"(g), "s"(lds_dst) : "memory");
}
#define WAIT_VM(n) asm volatile("s_waitcnt vmcnt(" #n ")" ::: "memory")
#define WAIT_LGKM0() asm volatile("s_waitcnt lgkmcnt(0)" ::: "memory")
#define RAW_BARRIER() do { asm volatile("" ::: "memory"); __builtin_amdgcn_s_barrier(); asm volatile("" ::: "memory"); } while (0)

#define STAMP_DECL
#define STAMP(i)
#define STAMP_FLUSH(k)
constexpr size_t WS_UR = 0;
constexpr size_t WS_XT = WS_UR + (size_t)D_ * L_ * 2;
constexpr size_t WS_HT = WS_XT + (size_t)D_ * L_ * 2;
constexpr size_t WS_KG = WS_HT + (size_t)D_ * L_ * 2;
constexpr size_t WS_HR = WS_KG + (size_t)D_ * L_ * 2;
constexpr size_t WS_KS = WS_HR + (size_t)D_ * L_ * 2;
constexpr size_t WS_CS = WS_KS + 4096;
constexpr size_t WS_H1 = WS_CS + 8192;

constexpr int K1_LN_BLOCKS = 128;
constexpr int K1_CS_BLOCKS = 28;
constexpr int K1_KG_BASE = K1_LN_BLOCKS + K1_CS_BLOCKS;
constexpr int KS_ = 260;
constexpr int K1_LDS = 48 * KS_ * 4;

struct cplx { float r, i; };
__device__ __forceinline__ cplx cmul(cplx a, cplx b) {
  cplx o; o.r = a.r * b.r - a.i * b.i; o.i = a.r * b.i + a.i * b.r; return o;
}
__device__ __forceinline__ cplx cexp_(float x, float y) {
  const float m = __expf(x);
  const float hi = y * 0.15915493667125702f;
  const float k = rintf(hi);
  float fr = fmaf(y, 0.15915493667125702f, -k);
  fr = fmaf(y, 6.4206382432985265e-09f, fr);
  cplx o; o.r = m * __builtin_amdgcn_cosf(fr); o.i = m * __builtin_amdgcn_sinf(fr); return o;
}
__device__ __forceinline__ int k_rowbase(int a) { return a < 2 ? 8 * a : a < 4 ? 8 + 4 * a : a < 8 ? 16 + 2 * a : 24 + a; }

__global__ __launch_bounds__(256, 3) void k1_prep(
    const uint16_t* __restrict__ x, const uint16_t* __restrict__ Are, const uint16_t* __restrict__ Aim,
    const uint16_t* __restrict__ Bre, const uint16_t* __restrict__ Bim, const uint16_t* __restrict__ Cre,
    const uint16_t* __restrict__ Cim, const uint16_t* __restrict__ logdt, const uint16_t* __restrict__ w1,
    char* __restrict__ ws) {
  extern __shared__ __attribute__((aligned(16))) char smem[];
  const int tid = threadIdx.x, lane = tid & 63, wv = tid >> 6;
  const int bid = blockIdx.x;
  STAMP_DECL
  if (bid >= K1_KG_BASE) {
    const int d = bid - K1_KG_BASE;
    float* Btab = (float*)smem;
    float* Atab = Btab + 16 * KS_;
    const int n = tid & 127, part = tid >> 7;
    const float dt = rbf(expf(bf2f(logdt[d])));
    const float zx = dt * bf2f(Are[d * N_ + n]);
    const float zy = dt * bf2f(Aim[d * N_ + n]);
    cplx c;
    {
      float br = dt * bf2f(Bre[d * N_ + n]), bi = dt * bf2f(Bim[d * N_ + n]);
      float cr = bf2f(Cre[d * N_ + n]), ci = bf2f(Cim[d * N_ + n]);
      c.r = cr * br - ci * bi; c.i = cr * bi + ci * br;
    }
    auto putA = [&](int row, cplx v) { *(f32x2*)(Atab + row * KS_ + 2 * n) = f32x2{v.r, -v.i}; };
    auto putB = [&](int row, cplx v) { *(f32x2*)(Btab + row * KS_ + 2 * n) = f32x2{v.r, v.i}; };
    STAMP(1);
    const cplx g1 = cexp_(128.f * zx, 128.f * zy);
    STAMP(2);
    cplx late[9];
    if (part == 0) {
      const cplx e1 = cexp_(zx, zy);
      const cplx e8 = cexp_(8.f * zx, 8.f * zy);
      cplx lo; lo.r = 1.f; lo.i = 0.f;
      putB(0, lo);
      lo = e8; putB(1, lo);
#pragma unroll
      for (int b = 2; b < 16; ++b) { lo = cmul(lo, e8); putB(b, lo); }
      cplx ce = c;
#pragma unroll
      for (int e = 0; e < 8; ++e) {
        putA(e, ce);
        putA(8 + e, cmul(ce, g1));
        if (e < 7) ce = cmul(ce, e1);
      }
#pragma unroll
      for (int a = 0; a < 9; ++a) { late[a].r = 0.f; late[a].i = 0.f; }
    } else {
      const cplx e2 = cexp_(2.f * zx, 2.f * zy);
      const cplx c2 = cmul(c, e2), c4 = cmul(c2, e2), c6 = cmul(c4, e2);
      cplx g = cmul(g1, g1);
#pragma unroll
      for (int a = 2; a < 4; ++a) {
        putA(16 + 4 * (a - 2) + 0, cmul(c, g));
        putA(16 + 4 * (a - 2) + 1, cmul(c2, g));
        putA(16 + 4 * (a - 2) + 2, cmul(c4, g));
        putA(16 + 4 * (a - 2) + 3, cmul(c6, g));
        g = cmul(g, g1);
      }
#pragma unroll
      for (int a = 4; a < 8; ++a) {
        putA(24 + 2 * (a - 4) + 0, cmul(c, g));
        putA(24 + 2 * (a - 4) + 1, cmul(c4, g));
        g = cmul(g, g1);
      }
#pragma unroll
      for (int a = 8; a < 17; ++a) {
        late[a - 8] = cmul(c, g);
        if (a < 16) g = cmul(g, g1);
      }
    }
    STAMP(3);
    __syncthreads();
    STAMP(4);
    f32x4 acc[3] = {f32x4{0, 0, 0, 0}, f32x4{0, 0, 0, 0}, f32x4{0, 0, 0, 0}};
    const int r16 = lane & 15, kk = lane >> 4;
    f32x4 bv[4];
#pragma unroll
    for (int gi = 0; gi < 4; ++gi) {
      const int k0 = (wv * 4 + gi) * 16 + 4 * kk;
      bv[gi] = *(const f32x4*)(Btab + r16 * KS_ + k0);
      f32x4 av[2];
#pragma unroll
      for (int r = 0; r < 2; ++r) av[r] = *(const f32x4*)(Atab + (16 * r + r16) * KS_ + k0);
#pragma unroll
      for (int t = 0; t < 4; ++t)
#pragma unroll
        for (int r = 0; r < 2; ++r)
          acc[r] = __builtin_amdgcn_mfma_f32_16x16x4f32(av[r][t], bv[gi][t], acc[r], 0, 0, 0);
    }
    STAMP(5);
    __syncthreads();
    if (part == 1) {
#pragma unroll
      for (int a = 0; a < 9; ++a) putA(a, late[a]);
    }
    __syncthreads();
#pragma unroll
    for (int gi = 0; gi < 4; ++gi) {
      const int k0 = (wv * 4 + gi) * 16 + 4 * kk;
      const f32x4 av = *(const f32x4*)(Atab + r16 * KS_ + k0);
#pragma unroll
      for (int t = 0; t < 4; ++t) acc[2] = __builtin_amdgcn_mfma_f32_16x16x4f32(av[t], bv[gi][t], acc[2], 0, 0, 0);
    }
    STAMP(6);
    float* Kv = Atab + 16 * KS_;
    {
      const int col = lane & 15, rq = lane >> 4;
#pragma unroll
      for (int r = 0; r < 3; ++r)
#pragma unroll
        for (int j = 0; j < 4; ++j) Kv[wv * 768 + (16 * r + 4 * rq + j) * 16 + col] = acc[r][j];
    }
    __syncthreads();
    STAMP(7);
    float* Kf = Btab;
    {
      float mx = 0.f;
#pragma unroll
      for (int i = 0; i < 3; ++i) {
        const int ix = tid + 256 * i;
        const float v = (Kv[ix] + Kv[768 + ix]) + (Kv[1536 + ix] + Kv[2304 + ix]);
        Kf[ix] = v;
        if (ix <= 640) mx = fmaxf(mx, fabsf(v));
      }
#pragma unroll
      for (int o = 32; o > 0; o >>= 1) mx = fmaxf(mx, __shfl_xor(mx, o));
      if (lane == 0) Kf[768 + wv] = mx;
    }
    __syncthreads();
    {
      STAMP(8);
      const float mx = fmaxf(fmaxf(Kf[768], Kf[769]), fmaxf(Kf[770], Kf[771]));
      int ex = (int)((__float_as_uint(mx) >> 23) & 255u) - 127;
      int sh = 14 - ex;
      sh = sh > 126 ? 126 : (sh < -126 ? -126 : sh);
      const float scale = __uint_as_float((uint32_t)(127 + sh) << 23);
      if (tid == 0) ((float*)(ws + WS_KS))[d] = __uint_as_float((uint32_t)(127 - sh) << 23);
      const int a = tid >> 4, b = tid & 15;
      const float* kp = Kf + k_rowbase(a) * 16 + b;
      const float vn = Kf[k_rowbase((tid + 1) >> 4) * 16 + ((tid + 1) & 15)] * scale;
      float v[8];
      if (a < 2) {
#pragma unroll
        for (int i = 0; i < 8; ++i) v[i] = kp[16 * i] * scale;
      } else if (a < 4) {
        const float w0 = kp[0] * scale, w1 = kp[16] * scale, w2 = kp[32] * scale, w3 = kp[48] * scale;
        v[0] = w0; v[1] = w0; v[2] = w1; v[3] = w2; v[4] = w2; v[5] = w2; v[6] = w3; v[7] = vn;
      } else if (a < 8) {
        const float w0 = kp[0] * scale, w1 = kp[16] * scale;
        v[0] = w0; v[1] = w0; v[2] = w0; v[3] = w1; v[4] = w1; v[5] = w1; v[6] = vn; v[7] = vn;
      } else {
        const float w0 = kp[0] * scale;
        v[0] = w0; v[1] = w0; v[2] = w0; v[3] = w0; v[4] = (tid & 1) ? vn : w0; v[5] = vn; v[6] = vn; v[7] = vn;
      }
      *(u32x4*)((uint16_t*)(ws + WS_KG) + (size_t)d * L_ + 8 * tid) =
          u32x4{pack2h(v[0], v[1]), pack2h(v[2], v[3]), pack2h(v[4], v[5]), pack2h(v[6], v[7])};
      STAMP(9);
    }
  } else if (bid < K1_LN_BLOCKS) {
    constexpr int TS = 1024 + 16;
    const int l0 = bid * 16;
    u32x4 xr[4];
#pragma unroll
    for (int i = 0; i < 4; ++i) xr[i] = *(const u32x4*)(x + (size_t)(l0 + wv * 4 + i) * D_ + lane * 8);
    float v[4][8], s[4], q[4];
#pragma unroll
    for (int i = 0; i < 4; ++i) {
      *(u32x4*)(smem + (16 + wv * 4 + i) * TS + lane * 16) = xr[i];
#pragma unroll
      for (int j = 0; j < 4; ++j) { v[i][2 * j] = bflo(xr[i][j]); v[i][2 * j + 1] = bfhi(xr[i][j]); }
      s[i] = ((v[i][0] + v[i][1]) + (v[i][2] + v[i][3])) + ((v[i][4] + v[i][5]) + (v[i][6] + v[i][7]));
    }
#pragma unroll
    for (int o = 32; o > 0; o >>= 1)
#pragma unroll
      for (int i = 0; i < 4; ++i) s[i] += __shfl_xor(s[i], o);
#pragma unroll
    for (int i = 0; i < 4; ++i) {
      s[i] *= (1.0f / D_);
      q[i] = 0.f;
#pragma unroll
      for (int j = 0; j < 8; ++j) { float dd = v[i][j] - s[i]; q[i] += dd * dd; }
    }
#pragma unroll
    for (int o = 32; o > 0; o >>= 1)
#pragma unroll
      for (int i = 0; i < 4; ++i) q[i] += __shfl_xor(q[i], o);
#pragma unroll
    for (int i = 0; i < 4; ++i) {
      const float m = rbf(s[i]), vb = rbf(q[i] * (1.0f / D_));
      const float sd = rbf(sqrtf(rbf(vb + 1e-5f)));
      const float r = 1.0f / sd;
      float uo[8];
#pragma unroll
      for (int j = 0; j < 8; ++j) {
        const float a = rbf(v[i][j] - m);
        float qq = a * r;
        qq = fmaf(fmaf(-qq, sd, a), r, qq);
        uo[j] = rbf(qq);
      }
      *(u32x4*)(smem + (wv * 4 + i) * TS + lane * 16) =
          u32x4{pack2h(uo[0], uo[1]), pack2h(uo[2], uo[3]), pack2h(uo[4], uo[5]), pack2h(uo[6], uo[7])};
    }
    STAMP(1);
    __syncthreads();
    STAMP(2);
#pragma unroll
    for (int pass = 0; pass < 2; ++pass) {
      uint32_t w[16];
#pragma unroll
      for (int r = 0; r < 16; ++r) w[r] = *(const uint32_t*)(smem + (pass * 16 + (pass == 0 ? 15 - r : r)) * TS + tid * 4);
      uint16_t* dst = (uint16_t*)(ws + (pass == 0 ? WS_UR : WS_XT));
      u32x4 a0, a1, b0, b1;
#pragma unroll
      for (int j = 0; j < 4; ++j) {
        a0[j] = (w[2 * j] & 0xffffu) | (w[2 * j + 1] << 16);
        b0[j] = (w[2 * j] >> 16) | (w[2 * j + 1] & 0xffff0000u);
        a1[j] = (w[8 + 2 * j] & 0xffffu) | (w[8 + 2 * j + 1] << 16);
        b1[j] = (w[8 + 2 * j] >> 16) | (w[8 + 2 * j + 1] & 0xffff0000u);
      }
      uint16_t* p0 = dst + (size_t)(2 * tid) * L_ + (pass == 0 ? (L_ - 16 - l0) : l0);
      *(u32x4*)(p0) = a0; *(u32x4*)(p0 + 8) = a1;
      *(u32x4*)(p0 + L_) = b0; *(u32x4*)(p0 + L_ + 8) = b1;
    }
    STAMP(3);
  } else {
    const int cb = bid - K1_LN_BLOCKS;
    const int c8 = tid & 7, rg = tid >> 3;
    u32x4 wr[16];
#pragma unroll
    for (int r = 0; r < 16; ++r) wr[r] = *(const u32x4*)(w1 + (size_t)(rg * 16 + r) * F_ + cb * 64 + c8 * 8);
    float s[8];
#pragma unroll
    for (int j = 0; j < 8; ++j) s[j] = 0.f;
#pragma unroll
    for (int r = 0; r < 16; ++r)
#pragma unroll
      for (int j = 0; j < 4; ++j) { s[2 * j] += bflo(wr[r][j]); s[2 * j + 1] += bfhi(wr[r][j]); }
    float* red = (float*)smem;
    *(f32x4*)(red + rg * 68 + c8 * 8) = f32x4{s[0], s[1], s[2], s[3]};
    *(f32x4*)(red + rg * 68 + c8 * 8 + 4) = f32x4{s[4], s[5], s[6], s[7]};
    __syncthreads();
    if (tid < 64) {
      float t = 0.f;
#pragma unroll
      for (int g = 0; g < 32; ++g) t += red[g * 68 + tid];
      ((float*)(ws + WS_CS))[cb * 64 + tid] = t;
    }
    STAMP(1);
  }
  STAMP_FLUSH(0);
}

constexpr int UC_S = 4160;
constexpr int UC_B4 = 4 * UC_S + 32;
constexpr int UREG = UC_B4 + 4 * UC_S;
constexpr int KOFF = UREG;
constexpr int CH_LDS = KOFF + 512 + 4096;
constexpr int K2_LDS = 2 * CH_LDS;

__global__ __launch_bounds__(256, 2) void k2_conv(const uint16_t* __restrict__ Dskip, char* __restrict__ ws) {
  extern __shared__ __attribute__((aligned(16))) char smem[];
  const int tid = threadIdx.x, lane = tid & 63;
  const int wv = __builtin_amdgcn_readfirstlane(tid >> 6);
  const int bid = blockIdx.x;
  STAMP_DECL
  const int chs = wv >> 1, half = wv & 1;
  const int d = 2 * bid + chs;
  const int tp = tid & 127;
  char* cb = smem + chs * CH_LDS;
  const unsigned lbase = (unsigned)(size_t)(lds_char*)smem + chs * CH_LDS;
  const uint16_t* uR = (const uint16_t*)(ws + WS_UR) + (size_t)d * L_;
  const uint16_t* xT = (const uint16_t*)(ws + WS_XT) + (size_t)d * L_;
  const uint16_t* Kg = (const uint16_t*)(ws + WS_KG) + (size_t)d * L_;
  const int m = lane & 15, kg = lane >> 4;
  u32x4 uu[4];
  uu[0] = *(const u32x4*)(uR + tp * 8);
  uu[1] = *(const u32x4*)(uR + tp * 8 + 8);
  uu[2] = *(const u32x4*)(uR + (tp + 128) * 8);
  uu[3] = *(const u32x4*)(uR + (tp < 127 ? tp + 129 : 255) * 8);
  glds16(Kg + (2 * half) * 512 + lane * 8, lbase + KOFF + 512 + (2 * half) * 1024);
  glds16(Kg + (2 * half + 1) * 512 + lane * 8, lbase + KOFF + 512 + (2 * half + 1) * 1024);
  u32x2 xe[4], ue[4];
#pragma unroll
  for (int j = 0; j < 4; ++j) {
    const int t0 = 256 * (4 * half + j) + 16 * m + 4 * kg;
    xe[j] = *(const u32x2*)(xT + t0);
    ue[j] = *(const u32x2*)(uR + (2044 - t0));
  }
  const float dsk = bf2f(Dskip[d]);
  const float kinv = ((const float*)(ws + WS_KS))[d];
  if (tp < 32) *(u32x4*)(cb + KOFF + tp * 16) = u32x4{0, 0, 0, 0};
  else if (tp < 40) {
    const int j = tp - 32;
    *(u32x4*)(cb + (j & 3) * UC_S + (j >> 2) * UC_B4 + 256 * 16) = u32x4{0, 0, 0, 0};
  }
  if (tp == 127) uu[3] = u32x4{0, 0, 0, 0};
#pragma unroll
  for (int it = 0; it < 2; ++it) {
    const uint32_t S[8] = {uu[2 * it][0], uu[2 * it][1], uu[2 * it][2], uu[2 * it][3],
                           uu[2 * it + 1][0], uu[2 * it + 1][1], uu[2 * it + 1][2], uu[2 * it + 1][3]};
#pragma unroll
    for (int ml = 0; ml < 8; ++ml) {
      const int s = 7 - ml;
      u32x4 o;
#pragma unroll
      for (int i = 0; i < 4; ++i)
        o[i] = (s & 1) ? __builtin_amdgcn_alignbit(S[(s >> 1) + i + 1], S[(s >> 1) + i], 16) : S[(s >> 1) + i];
      *(u32x4*)(cb + (ml & 3) * UC_S + (ml >> 2) * UC_B4 + (tp + 128 * it) * 16) = o;
    }
  }
  STAMP(1);
  WAIT_VM(0);
  __syncthreads();
  STAMP(2);

  const int ml = m & 7, mh = m >> 3;
  const char* abase = cb + (ml & 3) * UC_S + (ml >> 2) * UC_B4 + 16 * (253 - mh + kg) - 256 * half;
  const char* bbase = cb + KOFF + 512 + 32 * m + 16 * kg - 32 - 256 * half;
  f32x4 acc[8];
#pragma unroll
  for (int t = 0; t < 8; ++t) acc[t] = f32x4{0, 0, 0, 0};
  f16x8 af[2][8], bfr[2][8];
#pragma unroll
  for (int j = 0; j < 8; ++j) {
    af[0][j] = *(const f16x8*)(abase - 512 * j);
    bfr[0][j] = *(const f16x8*)(bbase + 512 * j);
  }
#pragma unroll
  for (int s = 0; s < 4; ++s) {
    if (s < 3) {
#pragma unroll
      for (int j = 0; j < 8; ++j) {
        af[(s + 1) & 1][j] = *(const f16x8*)(abase - 512 * j - 64 * (s + 1));
        bfr[(s + 1) & 1][j] = *(const f16x8*)(bbase + 512 * j - 64 * (s + 1));
      }
    }
#pragma unroll
    for (int T = 0; T < 8; ++T)
#pragma unroll
      for (int al = 0; al <= T; ++al)
        acc[T] = __builtin_amdgcn_mfma_f32_16x16x32_f16(af[s & 1][al], bfr[s & 1][T - al], acc[T], 0, 0, 0);
  }
  STAMP(3);
  __syncthreads();
  STAMP(4);
  f32x4 keep[4];
  if (half) {
#pragma unroll
    for (int j = 0; j < 4; ++j) { *(f32x4*)(cb + 4096 + j * 1024 + lane * 16) = acc[j]; keep[j] = acc[4 + j]; }
    asm volatile("" ::: "memory");
  } else {
#pragma unroll
    for (int j = 0; j < 4; ++j) { *(f32x4*)(cb + j * 1024 + lane * 16) = acc[4 + j]; keep[j] = acc[j]; }
    asm volatile("" ::: "memory");
  }
  __syncthreads();
  STAMP(5);
  uint16_t* hT = (uint16_t*)(ws + WS_HT) + (size_t)d * L_;
#pragma unroll
  for (int j = 0; j < 4; ++j) {
    const f32x4 o = *(const f32x4*)(cb + (1 - half) * 4096 + j * 1024 + lane * 16);
    const f32x4 a = keep[j];
    float hv[4];
#pragma unroll
    for (int r = 0; r < 4; ++r) {
      const float y = rbf((a[r] + o[r]) * kinv);
      const uint32_t uw = ue[j][1 - (r >> 1)];
      const f16x2 u2 = __builtin_bit_cast(f16x2, uw);
      const float uf = (float)u2[1 - (r & 1)];
      const uint32_t xw = xe[j][r >> 1];
      const float xf = (r & 1) ? bfhi(xw) : bflo(xw);
      hv[r] = xf + rbf(y + rbf(uf * dsk));
    }
    *(u32x2*)(hT + 256 * (4 * half + j) + 16 * m + 4 * kg) = u32x2{pack2(hv[0], hv[1]), pack2(hv[2], hv[3])};
  }
  STAMP(6);
  STAMP_FLUSH(1);
}

constexpr int G1_HS = 64 * 256;
constexpr int G1_WS = 64 * 224;
constexpr int G1_STAGE = G1_HS + G1_WS;
constexpr int G1_NS = 5;
constexpr int G1_ST = G1_NS * G1_STAGE;
constexpr int K3_LDS = G1_ST + 1024;

__device__ __forceinline__ bf16x4 tr_read(const char* p) {
  return __builtin_amdgcn_ds_read_tr16_b64_v4i16((lds_bf16x4*)p);
}
__device__ __forceinline__ bf16x8 cat8(bf16x4 a, bf16x4 b) {
  return bf16x8{a[0], a[1], a[2], a[3], b[0], b[1], b[2], b[3]};
}
__device__ __forceinline__ void swap16(uint32_t& a, uint32_t& b) {
  auto r = __builtin_amdgcn_permlane16_swap(a, b, false, false);
  a = r[0]; b = r[1];
}
typedef __attribute__((ext_vector_type(2))) __bf16 bf16v2;
__device__ __forceinline__ uint32_t cvt2(f32x2 v) {
  const bf16v2 b = __builtin_convertvector(v, bf16v2);
  return __builtin_bit_cast(uint32_t, b);
}
#define WAIT_VM_N(n) asm volatile("s_waitcnt vmcnt(%0)" :: "i"(n) : "memory")

template <int WF>
__device__ __forceinline__ void k3_body(const uint16_t* __restrict__ w1, char* __restrict__ ws, char* smem,
                                        const int lane, const int wv, const int l0, const int f0, const int ft) {
  constexpr int NT = WF ? 3 : 4;
  constexpr int CI0 = WF ? 4 : 0;
  STAMP_DECL
  const int wl = wv & 3;
  const unsigned lbase = (unsigned)(size_t)(lds_char*)smem;
  const uint16_t* hT = (const uint16_t*)(ws + WS_HT);
  const int g = lane >> 4, q = (lane >> 2) & 3, pp = lane & 3, r15 = lane & 15;
  int hoff[2], woff[2];
#pragma unroll
  for (int i = 0; i < 2; ++i) {
    const int row = (wv + 8 * i) * 4 + (lane >> 4);
    hoff[i] = row * L_ + l0 + 8 * ((lane & 15) ^ (2 * (row & 7)));
    const int pw = (wv + 8 * i < 14) ? wv + 8 * i : wv;
    const int nn = pw * 64 + lane;
    const int r2 = nn / 14, ch = nn - r2 * 14;
    woff[i] = r2 * F_ + f0 + 8 * ch;
  }
  const float* cs = (const float*)(ws + WS_CS);
  f32x4 c4[NT];
#pragma unroll
  for (int ci = 0; ci < NT; ++ci) c4[ci] = *(const f32x4*)(cs + f0 + 16 * (CI0 + ci) + 4 * g);
  auto stage = [&](int kt) {
    const unsigned sb = lbase + (kt % G1_NS) * G1_STAGE;
    const uint16_t* hs = hT + (size_t)kt * 64 * L_;
    const uint16_t* wsrc = w1 + (size_t)kt * 64 * F_;
    glds16(hs + hoff[0], sb + wv * 1024);
    glds16(wsrc + woff[0], sb + G1_HS + wv * 1024);
    glds16(hs + hoff[1], sb + (wv + 8) * 1024);
    if (wv < 6) glds16(wsrc + woff[1], sb + G1_HS + (wv + 8) * 1024);
  };
#pragma unroll
  for (int s = 0; s < 4; ++s) stage(s);

  const int xv = 2 * (4 * (g & 1) + q);
  int hrd[2];
#pragma unroll
  for (int cc = 0; cc < 2; ++cc)
    hrd[cc] = (4 * g + q) * 256 + ((((2 * (2 * wl + cc)) ^ xv) + (pp >> 1)) * 16) + 8 * (pp & 1);
  const int wrd = G1_HS + (4 * g + q) * 224 + 8 * pp + 32 * CI0;
  const int lrow = l0 + 32 * wl + r15;

  f32x4 acc[NT][2], accS[2], accG[2];
#pragma unroll
  for (int i = 0; i < NT; ++i) { acc[i][0] = f32x4{0, 0, 0, 0}; acc[i][1] = f32x4{0, 0, 0, 0}; }
  accS[0] = accS[1] = accG[0] = accG[1] = f32x4{0, 0, 0, 0};
  const bf16x8 ones = {0x3f80, 0x3f80, 0x3f80, 0x3f80, 0x3f80, 0x3f80, 0x3f80, 0x3f80};

  struct Frag { bf16x8 h[2], w[NT]; };
  auto ldfrag = [&](Frag& F, int kt, int ks) {
    const char* sb = smem + (kt % G1_NS) * G1_STAGE;
#pragma unroll
    for (int cc = 0; cc < 2; ++cc)
      F.h[cc] = cat8(tr_read(sb + hrd[cc] + ks * 8192), tr_read(sb + hrd[cc] + ks * 8192 + 4096));
#pragma unroll
    for (int ci = 0; ci < NT; ++ci)
      F.w[ci] = cat8(tr_read(sb + wrd + ks * 32 * 224 + 32 * ci), tr_read(sb + wrd + (ks * 32 + 16) * 224 + 32 * ci));
  };
  auto mma = [&](const Frag& F, int kstep) {
#pragma unroll
    for (int ci = 0; ci < NT; ++ci)
#pragma unroll
      for (int cc = 0; cc < 2; ++cc)
        acc[ci][cc] = __builtin_amdgcn_mfma_f32_16x16x32_bf16(F.w[ci], F.h[cc], acc[ci][cc], 0, 0, 0);
    if (WF) {
#pragma unroll
      for (int cc = 0; cc < 2; ++cc) {
        accS[cc] = __builtin_amdgcn_mfma_f32_16x16x32_bf16(ones, F.h[cc], accS[cc], 0, 0, 0);
        accG[cc] = __builtin_amdgcn_mfma_f32_16x16x32_bf16(F.h[cc], F.h[cc], accG[cc], 0, 0, 0);
      }
    } else if (kstep == ft) {
      asm volatile("" ::: "memory");
#pragma unroll
      for (int cc = 0; cc < 2; ++cc) {
        const u32x4 hv = __builtin_bit_cast(u32x4, F.h[cc]);
        uint32_t a0 = hv[0], a1 = hv[1], b0 = hv[2], b1 = hv[3];
        swap16(a0, b0); swap16(a1, b1);
        uint16_t* p = (uint16_t*)(ws + WS_HR) + (size_t)(lrow + 16 * cc) * D_ + 32 * ft + 16 * (g & 1) + 8 * (g >> 1);
        *(u32x4*)p = u32x4{a0, a1, b0, b1};
      }
    }
  };
#define K3_WAIT(r) do { if (wv < 6) { WAIT_VM_N(4 * (r)); } else { WAIT_VM_N(3 * (r)); } } while (0)
  Frag F0, F1;
  STAMP(1);
  K3_WAIT(3);
  RAW_BARRIER();
  STAMP(2);
  ldfrag(F0, 0, 0);
#pragma unroll
  for (int kt = 0; kt < 8; ++kt) {
    STAMP(3 + kt);
    ldfrag(F1, kt, 1);
    mma(F0, 2 * kt);
    if (kt < 7) {
      if (kt < 5) K3_WAIT(2);
      else if (kt == 5) K3_WAIT(1);
      else K3_WAIT(0);
      RAW_BARRIER();
      if (kt + 4 < 8) stage(kt + 4);
      ldfrag(F0, kt + 1, 0);
    }
    mma(F1, 2 * kt + 1);
  }
  STAMP(11);
  float* stt = (float*)(smem + G1_ST);
  float mm[2], rs[2];
  if (WF) {
#pragma unroll
    for (int cc = 0; cc < 2; ++cc) {
      const float s = accS[cc][0];
      float dg = (pp == 0) ? accG[cc][0] : (pp == 1) ? accG[cc][1] : (pp == 2) ? accG[cc][2] : accG[cc][3];
      const float sq = __shfl(dg, r15 + 16 * (r15 >> 2));
      const float mean = s * (1.0f / D_);
      const float var = fmaxf(sq * (1.0f / D_) - mean * mean, 0.f);
      mm[cc] = rbf(mean);
      const float sd = rbf(sqrtf(rbf(rbf(var) + 1e-5f)));
      rs[cc] = 1.0f / sd;
      if (g == 0) *(f32x2*)(stt + 2 * (32 * wl + 16 * cc + r15)) = f32x2{mm[cc], rs[cc]};
    }
  }
  __syncthreads();
  __builtin_amdgcn_sched_barrier(0);
  if (!WF) {
#pragma unroll
    for (int cc = 0; cc < 2; ++cc) {
      const f32x2 t = *(const f32x2*)(stt + 2 * (32 * wl + 16 * cc + r15));
      mm[cc] = t[0]; rs[cc] = t[1];
    }
  }
  uint16_t* H1 = (uint16_t*)(ws + WS_H1);
  uint32_t pk[NT][2][2];
#pragma unroll
  for (int cc = 0; cc < 2; ++cc)
#pragma unroll
    for (int ci = 0; ci < NT; ++ci)
#pragma unroll
      for (int j2 = 0; j2 < 2; ++j2) {
        const f32x2 a = {acc[ci][cc][2 * j2], acc[ci][cc][2 * j2 + 1]};
        const f32x2 c = {c4[ci][2 * j2], c4[ci][2 * j2 + 1]};
        const uint32_t vb = cvt2((a - mm[cc] * c) * rs[cc]);
        const f32x2 v = {bflo(vb), bfhi(vb)};
        const f32x2 e = v * -1.4426950408889634f;
        const f32x2 dn = f32x2{__builtin_amdgcn_exp2f(e[0]), __builtin_amdgcn_exp2f(e[1])} + 1.0f;
        const f32x2 o = v * f32x2{__builtin_amdgcn_rcpf(dn[0]), __builtin_amdgcn_rcpf(dn[1])};
        pk[ci][cc][j2] = cvt2(o);
      }
  STAMP(12);
#pragma unroll
  for (int cc = 0; cc < 2; ++cc)
#pragma unroll
    for (int cp = 0; cp < NT / 2; ++cp) {
      swap16(pk[2 * cp][cc][0], pk[2 * cp + 1][cc][0]);
      swap16(pk[2 * cp][cc][1], pk[2 * cp + 1][cc][1]);
      uint16_t* p = H1 + (size_t)(lrow + 16 * cc) * F_ + f0 + 16 * (CI0 + 2 * cp + (g & 1)) + 8 * (g >> 1);
      *(u32x4*)p = u32x4{pk[2 * cp][cc][0], pk[2 * cp][cc][1], pk[2 * cp + 1][cc][0], pk[2 * cp + 1][cc][1]};
    }
  if (WF) {
    swap16(pk[NT - 1][0][0], pk[NT - 1][1][0]);
    swap16(pk[NT - 1][0][1], pk[NT - 1][1][1]);
    uint16_t* p = H1 + (size_t)(lrow + 16 * (g & 1)) * F_ + f0 + 96 + 8 * (g >> 1);
    *(u32x4*)p = u32x4{pk[NT - 1][0][0], pk[NT - 1][0][1], pk[NT - 1][1][0], pk[NT - 1][1][1]};
  }
  STAMP(13);
  STAMP_FLUSH(2);
}

__global__ __launch_bounds__(512) void k3_gemm1(const uint16_t* __restrict__ w1, char* __restrict__ ws) {
  extern __shared__ __attribute__((aligned(16))) char smem[];
  const int tid = threadIdx.x, lane = tid & 63;
  const int wv = __builtin_amdgcn_readfirstlane(tid >> 6);
  const int bid = blockIdx.x;
  const int xcd = bid & 7, idx = bid >> 3;
  const int lt = 4 * (xcd >> 1) + (idx & 3), ft = 8 * (xcd & 1) + (idx >> 2);
  if (wv < 4) k3_body<0>(w1, ws, smem, lane, wv, lt * 128, ft * 112, ft);
  else k3_body<1>(w1, ws, smem, lane, wv, lt * 128, ft * 112, ft);
}

constexpr int G2_HS = 64 * 256;
constexpr int G2_WS = 128 * 128;
constexpr int G2_STAGE = G2_HS + G2_WS;
constexpr int G2_NS = 5;
constexpr int G2_NK = F_ / 128;
constexpr int K4_LDS = G2_NS * G2_STAGE;

__global__ __launch_bounds__(512) void k4_gemm2(const uint16_t* __restrict__ w2, char* __restrict__ ws,
                                                uint16_t* __restrict__ out) {
  extern __shared__ __attribute__((aligned(16))) char smem[];
  const int tid = threadIdx.x, lane = tid & 63;
  const int wv = __builtin_amdgcn_readfirstlane(tid >> 6);
  const int bid = blockIdx.x;
  const int xcd = bid & 7, idx = bid >> 3;
  STAMP_DECL
  const int lt = 4 * xcd + (idx & 3), dtile = idx >> 2;
  const int l0 = lt * 64, d0 = dtile * 64;
  const unsigned lbase = (unsigned)(size_t)(lds_char*)smem;
  const uint16_t* H1 = (const uint16_t*)(ws + WS_H1);
  const int g = lane >> 4, q = (lane >> 2) & 3, pp = lane & 3;
  const int wl = wv & 3, wd = wv >> 2;
  const int r15 = lane & 15;
  int hoff[2], woff[2];
#pragma unroll
  for (int i = 0; i < 2; ++i) {
    const int row = (wv * 2 + i) * 4 + (lane >> 4);
    hoff[i] = (l0 + row) * F_ + 8 * ((lane & 15) ^ (row & 15));
    const int r2 = (wv * 2 + i) * 8 + (lane >> 3);
    const int gf = ((r2 >> 1) & 1) | (((r2 >> 3) & 1) << 1);
    woff[i] = r2 * D_ + d0 + 8 * ((lane & 7) ^ (2 * gf));
  }
  const size_t oo = (size_t)(l0 + 16 * wl + r15) * D_ + d0 + 32 * wd + 16 * (g & 1) + 8 * (g >> 1);
  const u32x4 hres = *(const u32x4*)((const uint16_t*)(ws + WS_HR) + oo);
  auto stage = [&](int kt) {
    const unsigned sb = lbase + (kt % G2_NS) * G2_STAGE;
#pragma unroll
    for (int i = 0; i < 2; ++i) {
      glds16(w2 + woff[i] + (size_t)kt * 128 * D_, sb + G2_HS + (wv * 2 + i) * 1024);
      glds16(H1 + hoff[i] + kt * 128, sb + (wv * 2 + i) * 1024);
    }
  };
#pragma unroll
  for (int s = 0; s < 4; ++s) stage(s);

  const int hrd = (16 * wl + r15) * 256;
  int wrd[2][2];
#pragma unroll
  for (int ci = 0; ci < 2; ++ci)
#pragma unroll
    for (int t = 0; t < 2; ++t) {
      const int row = 8 * g + 4 * t + q;
      const int gf = ((row >> 1) & 1) | (((row >> 3) & 1) << 1);
      wrd[ci][t] = G2_HS + row * 128 + (((2 * (2 * wd + ci)) ^ (2 * gf)) + (pp >> 1)) * 16 + 8 * (pp & 1);
    }
  f32x4 acc[2] = {f32x4{0, 0, 0, 0}, f32x4{0, 0, 0, 0}};

  struct Frag { bf16x8 h[2], w[2][2]; };
  auto ldfrag = [&](Frag& F, int kt, int kh) {
    const char* sb = smem + (kt % G2_NS) * G2_STAGE;
#pragma unroll
    for (int k2 = 0; k2 < 2; ++k2) {
      const int ks = 2 * kh + k2;
      F.h[k2] = *(const bf16x8*)(sb + hrd + (((4 * ks + g) ^ r15) * 16));
#pragma unroll
      for (int ci = 0; ci < 2; ++ci)
        F.w[k2][ci] = cat8(tr_read(sb + wrd[ci][0] + ks * 4096), tr_read(sb + wrd[ci][1] + ks * 4096));
    }
  };
  auto mma = [&](const Frag& F) {
#pragma unroll
    for (int k2 = 0; k2 < 2; ++k2)
#pragma unroll
      for (int ci = 0; ci < 2; ++ci)
        acc[ci] = __builtin_amdgcn_mfma_f32_16x16x32_bf16(F.w[k2][ci], F.h[k2], acc[ci], 0, 0, 0);
  };
  Frag F0, F1;
  STAMP(1);
  WAIT_VM(12);
  RAW_BARRIER();
  STAMP(2);
  ldfrag(F0, 0, 0);
#pragma unroll
  for (int kt = 0; kt < G2_NK; ++kt) {
    if ((kt & 1) == 0) STAMP(3 + kt / 2);
    ldfrag(F1, kt, 1);
    mma(F0);
    if (kt < G2_NK - 1) {
      if (kt < G2_NK - 3) WAIT_VM(8);
      else if (kt == G2_NK - 3) WAIT_VM(4);
      else WAIT_VM(0);
      RAW_BARRIER();
      if (kt + 4 < G2_NK) stage(kt + 4);
      ldfrag(F0, kt + 1, 0);
    }
    mma(F1);
  }
  STAMP(10);
  {
    uint32_t a0 = pack2(acc[0][0], acc[0][1]), a1 = pack2(acc[0][2], acc[0][3]);
    uint32_t b0 = pack2(acc[1][0], acc[1][1]), b1 = pack2(acc[1][2], acc[1][3]);
    swap16(a0, b0); swap16(a1, b1);
    const uint32_t v[4] = {a0, a1, b0, b1};
    u32x4 r;
#pragma unroll
    for (int j = 0; j < 4; ++j) r[j] = pack2(bflo(hres[j]) + bflo(v[j]), bfhi(hres[j]) + bfhi(v[j]));
    *(u32x4*)(out + oo) = r;
  }
  STAMP(11);
  STAMP_FLUSH(3);
}

extern "C" void kernel_launch(void* const* d_in, const int* in_sizes, int n_in,
                              void* d_out, int out_size, void* d_ws, size_t ws_size,
                              hipStream_t stream) {
  const uint16_t* x = (const uint16_t*)d_in[0];
  const uint16_t* Are = (const uint16_t*)d_in[1];
  const uint16_t* Aim = (const uint16_t*)d_in[2];
  const uint16_t* Bre = (const uint16_t*)d_in[3];
  const uint16_t* Bim = (const uint16_t*)d_in[4];
  const uint16_t* Cre = (const uint16_t*)d_in[5];
  const uint16_t* Cim = (const uint16_t*)d_in[6];
  const uint16_t* logdt = (const uint16_t*)d_in[7];
  const uint16_t* Dskip = (const uint16_t*)d_in[8];
  const uint16_t* w1 = (const uint16_t*)d_in[10];
  const uint16_t* w2 = (const uint16_t*)d_in[11];
  char* ws = (char*)d_ws;
  (void)hipFuncSetAttribute((const void*)k1_prep, hipFuncAttributeMaxDynamicSharedMemorySize, K1_LDS);
  (void)hipFuncSetAttribute((const void*)k2_conv, hipFuncAttributeMaxDynamicSharedMemorySize, K2_LDS);
  (void)hipFuncSetAttribute((const void*)k3_gemm1, hipFuncAttributeMaxDynamicSharedMemorySize, K3_LDS);
  (void)hipFuncSetAttribute((const void*)k4_gemm2, hipFuncAttributeMaxDynamicSharedMemorySize, K4_LDS);
  k1_prep<<<K1_KG_BASE + D_, 256, K1_LDS, stream>>>(x, Are, Aim, Bre, Bim, Cre, Cim, logdt, w1, ws);
  k2_conv<<<D_ / 2, 256, K2_LDS, stream>>>(Dskip, ws);
  k3_gemm1<<<256, 512, K3_LDS, stream>>>(w1, ws);
  k4_gemm2<<<256, 512, K4_LDS, stream>>>(w2, ws, (uint16_t*)d_out);
}
```

```cpp
#include <hip/hip_runtime.h>
#include <hip/hip_bf16.h>
#include <stdint.h>
#include <type_traits>

constexpr int L_ = 2048, D_ = 512, N_ = 128, F_ = 1792;

typedef __attribute__((ext_vector_type(8))) short bf16x8;
typedef __attribute__((ext_vector_type(4))) short bf16x4;
typedef __attribute__((ext_vector_type(2))) short bf16x2_t;
typedef __attribute__((ext_vector_type(8))) _Float16 f16x8;
typedef __attribute__((ext_vector_type(2))) _Float16 f16x2;
typedef __attribute__((ext_vector_type(4))) float f32x4;
typedef __attribute__((ext_vector_type(2))) float f32x2;
typedef __attribute__((ext_vector_type(4))) unsigned u32x4;
typedef __attribute__((ext_vector_type(2))) unsigned u32x2;
typedef __attribute__((address_space(3))) bf16x4 lds_bf16x4;
typedef __attribute__((address_space(3))) char lds_char;

__device__ __forceinline__ float bf2f(uint16_t v) { return __uint_as_float(((uint32_t)v) << 16); }
__device__ __forceinline__ float bflo(uint32_t v) { return __uint_as_float(v << 16); }
__device__ __forceinline__ float bfhi(uint32_t v) { return __uint_as_float(v & 0xffff0000u); }
__device__ __forceinline__ uint16_t f2bf(float f) {
  __hip_bfloat16 b = __float2bfloat16(f);
  return *reinterpret_cast<uint16_t*>(&b);
}
__device__ __forceinline__ float rbf(float f) { return bf2f(f2bf(f)); }
__device__ __forceinline__ uint32_t pack2(float lo, float hi) { return (uint32_t)f2bf(lo) | ((uint32_t)f2bf(hi) << 16); }
__device__ __forceinline__ uint32_t pack2h(float lo, float hi) {
  f16x2 v; v[0] = (_Float16)lo; v[1] = (_Float16)hi;
  return __builtin_bit_cast(uint32_t, v);
}

__device__ __forceinline__ void glds16(const void* g, unsigned lds_dst) {
  unsigned keep;
  asm volatile("s_mov_b32 %0, m0\n\ts_mov_b32 m0, %2\n\ts_nop 0\n\tglobal_load_lds_dwordx4 %1, off\n\ts_mov_b32 m0, %0"
               : "=&s"(keep) : "v"(g), "s"(lds_dst) : "memory");
}
#define WAIT_VM(n) asm volatile("s_waitcnt vmcnt(" #n ")" ::: "memory")
#define WAIT_LGKM0() asm volatile("s_waitcnt lgkmcnt(0)" ::: "memory")
#define RAW_BARRIER() do { asm volatile("" ::: "memory"); __builtin_amdgcn_s_barrier(); asm volatile("" ::: "memory"); } while (0)

#define STAMP_DECL
#define STAMP(i)
#define STAMP_FLUSH(k)
constexpr size_t WS_UR = 0;
constexpr size_t WS_XT = WS_UR + (size_t)D_ * L_ * 2;
constexpr size_t WS_HT = WS_XT + (size_t)D_ * L_ * 2;
constexpr size_t WS_KG = WS_HT + (size_t)D_ * L_ * 2;
constexpr size_t WS_HR = WS_KG + (size_t)D_ * L_ * 2;
constexpr size_t WS_KS = WS_HR + (size_t)D_ * L_ * 2;
constexpr size_t WS_CS = WS_KS + 4096;
constexpr size_t WS_H1 = WS_CS + 8192;

constexpr int T_RS = 528;
constexpr int T_LO = 57 * T_RS;
constexpr int CH_T = 2 * T_LO;
constexpr int K1_RES = 2 * CH_T;
constexpr int K1_LN = K1_RES + 2 * 3104;
constexpr int K1_TS = 1040;
constexpr int K1_DUMP = K1_LN + 16 * K1_TS;
constexpr int K1_LDS = K1_DUMP + 1024;

typedef __attribute__((ext_vector_type(2))) __bf16 bf16v2;
__device__ __forceinline__ uint32_t cvt2(f32x2 v) {
  const bf16v2 b = __builtin_convertvector(v, bf16v2);
  return __builtin_bit_cast(uint32_t, b);
}
__device__ __forceinline__ f32x2 cmul(f32x2 a, f32x2 b) {
  const f32x2 t = f32x2{a.x, a.x} * b;
  return __builtin_elementwise_fma(f32x2{a.y, a.y}, f32x2{-b.y, b.x}, t);
}
__device__ __forceinline__ f32x2 cexp_(float x, float y) {
  const float m = __expf(x);
  const float hi = y * 0.15915493667125702f;
  const float k = rintf(hi);
  float fr = fmaf(y, 0.15915493667125702f, -k);
  fr = fmaf(y, 6.4206382432985265e-09f, fr);
  return f32x2{m * __builtin_amdgcn_cosf(fr), m * __builtin_amdgcn_sinf(fr)};
}
__device__ __forceinline__ int k_rowbase(int a) { return a < 2 ? 8 * a : a < 4 ? 8 + 4 * a : a < 8 ? 16 + 2 * a : 24 + a; }
template <int C> __device__ __forceinline__ float dppf(float v) {
  return __builtin_bit_cast(float, __builtin_amdgcn_update_dpp(0, __builtin_bit_cast(int, v), C, 0xf, 0xf, true));
}
__device__ __forceinline__ float wave_sum(float v) {
  v += dppf<0xB1>(v); v += dppf<0x4E>(v); v += dppf<0x141>(v); v += dppf<0x140>(v);
  { const auto r = __builtin_amdgcn_permlane16_swap(__float_as_uint(v), __float_as_uint(v), false, false);
    v = __uint_as_float(r[0]) + __uint_as_float(r[1]); }
  { const auto r = __builtin_amdgcn_permlane32_swap(__float_as_uint(v), __float_as_uint(v), false, false);
    v = __uint_as_float(r[0]) + __uint_as_float(r[1]); }
  return v;
}
__device__ __forceinline__ float wave_max(float v) {
  v = fmaxf(v, dppf<0xB1>(v)); v = fmaxf(v, dppf<0x4E>(v)); v = fmaxf(v, dppf<0x141>(v)); v = fmaxf(v, dppf<0x140>(v));
  { const auto r = __builtin_amdgcn_permlane16_swap(__float_as_uint(v), __float_as_uint(v), false, false);
    v = fmaxf(__uint_as_float(r[0]), __uint_as_float(r[1])); }
  { const auto r = __builtin_amdgcn_permlane32_swap(__float_as_uint(v), __float_as_uint(v), false, false);
    v = fmaxf(__uint_as_float(r[0]), __uint_as_float(r[1])); }
  return v;
}

__global__ __launch_bounds__(512) void k1_prep(
    const uint16_t* __restrict__ x, const uint16_t* __restrict__ Are, const uint16_t* __restrict__ Aim,
    const uint16_t* __restrict__ Bre, const uint16_t* __restrict__ Bim, const uint16_t* __restrict__ Cre,
    const uint16_t* __restrict__ Cim, const uint16_t* __restrict__ logdt, const uint16_t* __restrict__ w1,
    const uint16_t* __restrict__ w2, char* __restrict__ ws) {
  extern __shared__ __attribute__((aligned(16))) char smem[];
  const int tid = threadIdx.x, lane = tid & 63;
  const int wv = __builtin_amdgcn_readfirstlane(tid >> 6);
  const int bid = blockIdx.x;
  STAMP_DECL
  const int ch = wv >> 2, wq = wv & 3, part = wq >> 1;
  const int t8 = tid & 255, n = tid & 127;
  const int d = 2 * bid + ch;
  const int l0 = ((bid & 7) * 32 + (bid >> 3)) * 8;
  char* tb = smem + ch * CH_T;
  const uint16_t pAr = Are[d * N_ + n], pAi = Aim[d * N_ + n], pBr = Bre[d * N_ + n], pBi = Bim[d * N_ + n],
                 pCr = Cre[d * N_ + n], pCi = Cim[d * N_ + n], pdt = logdt[d];
  const u32x4 xr = *(const u32x4*)(x + (size_t)(l0 + wv) * D_ + lane * 8);
  {
    const float dt = rbf(expf(bf2f(pdt)));
    const float zx = dt * bf2f(pAr);
    const float zy = dt * bf2f(pAi);
    f32x2 c;
    {
      const float br = dt * bf2f(pBr), bi = dt * bf2f(pBi);
      const float cr = bf2f(pCr), ci = bf2f(pCi);
      c = f32x2{cr * br - ci * bi, -(cr * bi + ci * br)};
    }
    auto putn = [&](auto NV, const int* rows, const f32x2* v) {
      constexpr int nv = decltype(NV)::value;
      uint32_t H[nv], Lw[nv];
      f32x2 hf[nv];
#pragma unroll
      for (int i = 0; i < nv; ++i) H[i] = cvt2(v[i]);
#pragma unroll
      for (int i = 0; i < nv; ++i) hf[i] = f32x2{bflo(H[i]), bfhi(H[i])};
#pragma unroll
      for (int i = 0; i < nv; ++i) hf[i] = v[i] - hf[i];
#pragma unroll
      for (int i = 0; i < nv; ++i) Lw[i] = cvt2(hf[i]);
#pragma unroll
      for (int i = 0; i < nv; ++i) {
        *(uint32_t*)(tb + rows[i] * T_RS + 4 * n) = H[i];
        *(uint32_t*)(tb + T_LO + rows[i] * T_RS + 4 * n) = Lw[i];
      }
    };
    auto cmuln = [&](auto NV, f32x2* o, const f32x2* v, f32x2 m) {
      constexpr int nv = decltype(NV)::value;
      const f32x2 mn = f32x2{-m.y, m.x};
      f32x2 t[nv];
#pragma unroll
      for (int i = 0; i < nv; ++i) t[i] = f32x2{v[i].x, v[i].x} * m;
#pragma unroll
      for (int i = 0; i < nv; ++i) o[i] = __builtin_elementwise_fma(f32x2{v[i].y, v[i].y}, mn, t[i]);
    };
    using I4 = std::integral_constant<int, 4>;
    using I8 = std::integral_constant<int, 8>;
    using I6 = std::integral_constant<int, 6>;
    using I3 = std::integral_constant<int, 3>;
    STAMP(1);
    const f32x2 g1 = cexp_(128.f * zx, -128.f * zy);
    if (part == 0) {
      const f32x2 e1 = cexp_(zx, -zy);
      const f32x2 e8 = cexp_(8.f * zx, 8.f * zy);
      const f32x2 e2 = cmul(e1, e1), e16 = cmul(e8, e8);
      const f32x2 e4 = cmul(e2, e2), e32 = cmul(e16, e16);
      f32x2 A[8], Bv[4];
      A[0] = c; A[1] = cmul(c, e1); A[2] = cmul(c, e2);
      Bv[0] = f32x2{1.f, 0.f}; Bv[1] = e8; Bv[2] = e16; Bv[3] = cmul(e16, e8);
      A[3] = cmul(A[1], e2);
      cmuln(I4{}, A + 4, A, e4);
      { const int r[4] = {41, 42, 43, 44}; putn(I4{}, r, Bv); }
      cmuln(I4{}, Bv, Bv, e32);
      { const int r[8] = {0, 1, 2, 3, 4, 5, 6, 7}; putn(I8{}, r, A); }
      cmuln(I8{}, A, A, g1);
      { const int r[4] = {45, 46, 47, 48}; putn(I4{}, r, Bv); }
      cmuln(I4{}, Bv, Bv, e32);
      { const int r[8] = {8, 9, 10, 11, 12, 13, 14, 15}; putn(I8{}, r, A); }
      { const int r[4] = {49, 50, 51, 52}; putn(I4{}, r, Bv); }
      cmuln(I4{}, Bv, Bv, e32);
      { const int r[4] = {53, 54, 55, 56}; putn(I4{}, r, Bv); }
    } else {
      const f32x2 e2 = cexp_(2.f * zx, -2.f * zy);
      const f32x2 g2 = cmul(g1, g1), e4 = cmul(e2, e2);
      const f32x2 g4 = cmul(g2, g2);
      f32x2 Q[8], R[6], S[4];
      Q[0] = cmul(c, g2);
      Q[1] = cmul(Q[0], g1); Q[2] = cmul(Q[0], g2);
      Q[3] = cmul(Q[2], g1);
      cmuln(I4{}, Q + 4, Q, g4);
      cmuln(I6{}, R, Q, e4);
      { f32x2 t[4] = {Q[0], R[0], Q[1], R[1]}; cmuln(I4{}, S, t, e2); }
      { const int r[8] = {16, 20, 24, 26, 28, 30, 32, 33}; putn(I8{}, r, Q); }
      cmuln(I4{}, Q, Q + 4, g4);
      { const int r[6] = {18, 22, 25, 27, 29, 31}; putn(I6{}, r, R); }
      cmuln(I3{}, Q + 4, Q, g4);
      { const int r[4] = {17, 19, 21, 23}; putn(I4{}, r, S); }
      { const int r[4] = {34, 35, 36, 37}; putn(I4{}, r, Q); }
      { const int r[3] = {38, 39, 40}; putn(I3{}, r, Q + 4); }
    }
  }
  STAMP(2);
  {
    char* ln = smem + K1_LN;
    *(u32x4*)(ln + (8 + wv) * K1_TS + lane * 16) = xr;
    f32x2 v[4];
#pragma unroll
    for (int j = 0; j < 4; ++j) v[j] = f32x2{bflo(xr[j]), bfhi(xr[j])};
    const f32x2 s2 = (v[0] + v[1]) + (v[2] + v[3]);
    const float mean = wave_sum(s2.x + s2.y) * (1.0f / D_);
    f32x2 q2 = f32x2{0.f, 0.f};
#pragma unroll
    for (int j = 0; j < 4; ++j) { const f32x2 dd = v[j] - mean; q2 = __builtin_elementwise_fma(dd, dd, q2); }
    const float var = wave_sum(q2.x + q2.y) * (1.0f / D_);
    const float m = rbf(mean);
    const float sd = rbf(sqrtf(rbf(rbf(var) + 1e-5f)));
    const float r = 1.0f / sd;
    u32x4 uo;
#pragma unroll
    for (int j = 0; j < 4; ++j) {
      const uint32_t a = cvt2(v[j] - m);
      const uint32_t u = cvt2(f32x2{bflo(a), bfhi(a)} * r);
      uo[j] = pack2h(bflo(u), bfhi(u));
    }
    *(u32x4*)(ln + wv * K1_TS + lane * 16) = uo;
  }
  {
    const int gi = min(bid * 512 + tid, D_ * F_ / 8 - 1);
    const unsigned dump = (unsigned)(size_t)(lds_char*)smem + K1_DUMP;
    glds16(w1 + (size_t)gi * 8, dump);
    glds16(w2 + (size_t)gi * 8, dump);
  }
  STAMP(3);
  __syncthreads();
  STAMP(4);
  {
    const char* ln = smem + K1_LN + ch * 8 * K1_TS + t8 * 4;
    uint32_t w[8];
#pragma unroll
    for (int r = 0; r < 8; ++r) w[r] = *(const uint32_t*)(ln + (ch == 0 ? 7 - r : r) * K1_TS);
    u32x4 a0, b0;
#pragma unroll
    for (int j = 0; j < 4; ++j) {
      a0[j] = __builtin_amdgcn_perm(w[2 * j + 1], w[2 * j], 0x05040100u);
      b0[j] = __builtin_amdgcn_perm(w[2 * j + 1], w[2 * j], 0x07060302u);
    }
    uint16_t* p0 = (uint16_t*)(ws + (ch == 0 ? WS_UR : WS_XT)) + (size_t)(2 * t8) * L_ + (ch == 0 ? (L_ - 8 - l0) : l0);
    *(u32x4*)(p0) = a0;
    *(u32x4*)(p0 + L_) = b0;
  }
  STAMP(5);
  float* Kf = (float*)(smem + K1_RES + ch * 3104);
  if (wq < 3) {
    const int r16 = lane & 15, kg = lane >> 4;
    const char* ta = tb + (16 * wq + r16) * T_RS + kg * 16;
    const char* tq = tb + (41 + r16) * T_RS + kg * 16;
    f32x4 a1 = f32x4{0, 0, 0, 0}, a2 = f32x4{0, 0, 0, 0};
    bf16x8 Ah[8], Al[8], Bh[8], Bl[8];
#pragma unroll
    for (int s = 0; s < 8; ++s) {
      Ah[s] = *(const bf16x8*)(ta + 64 * s); Bh[s] = *(const bf16x8*)(tq + 64 * s);
      Bl[s] = *(const bf16x8*)(tq + T_LO + 64 * s); Al[s] = *(const bf16x8*)(ta + T_LO + 64 * s);
    }
    __builtin_amdgcn_sched_barrier(0);
#pragma unroll
    for (int s = 0; s < 8; ++s) {
      a1 = __builtin_amdgcn_mfma_f32_16x16x32_bf16(Ah[s], Bh[s], a1, 0, 0, 0);
      a2 = __builtin_amdgcn_mfma_f32_16x16x32_bf16(Ah[s], Bl[s], a2, 0, 0, 0);
      a2 = __builtin_amdgcn_mfma_f32_16x16x32_bf16(Al[s], Bh[s], a2, 0, 0, 0);
    }
    a1 += a2;
    float mx = 0.f;
#pragma unroll
    for (int j = 0; j < 4; ++j) {
      const int ix = (16 * wq + 4 * kg + j) * 16 + r16;
      Kf[ix] = a1[j];
      if (ix <= 640) mx = fmaxf(mx, fabsf(a1[j]));
    }
    mx = wave_max(mx);
    if (lane == 0) Kf[768 + wq] = mx;
  }
  STAMP(6);
  __syncthreads();
  STAMP(7);
  {
    const int a = t8 >> 4, b = t8 & 15;
    const float* kp = Kf + k_rowbase(a) * 16 + b;
    float kv[8];
#pragma unroll
    for (int i = 0; i < 8; ++i) kv[i] = kp[16 * i];
    float vn = Kf[k_rowbase((t8 + 1) >> 4) * 16 + ((t8 + 1) & 15)];
    const f32x4 m4 = *(const f32x4*)(Kf + 768);
    __builtin_amdgcn_sched_barrier(0);
    const float mx = fmaxf(fmaxf(m4[0], m4[1]), m4[2]);
    const int ex = (int)((__float_as_uint(mx) >> 23) & 255u) - 127;
    int sh = 14 - ex;
    sh = sh > 126 ? 126 : (sh < -126 ? -126 : sh);
    const float scale = __uint_as_float((uint32_t)(127 + sh) << 23);
    if (t8 == 0) ((float*)(ws + WS_KS))[d] = __uint_as_float((uint32_t)(127 - sh) << 23);
    float v[8];
    {
      const bool c2 = a < 2, c4 = a < 4, c8 = a < 8;
      const float w0 = kv[0], w1_ = kv[1], w2_ = kv[2], w3 = kv[3];
      v[0] = w0;
      v[1] = c2 ? w1_ : w0;
      v[2] = c2 ? w2_ : c4 ? w1_ : w0;
      v[3] = c2 ? w3 : c4 ? w2_ : c8 ? w1_ : w0;
      v[4] = c2 ? kv[4] : c4 ? w2_ : c8 ? w1_ : ((t8 & 1) ? vn : w0);
      v[5] = c2 ? kv[5] : c4 ? w2_ : c8 ? w1_ : vn;
      v[6] = c2 ? kv[6] : c4 ? w3 : vn;
      v[7] = c2 ? kv[7] : vn;
#pragma unroll
      for (int i = 0; i < 8; ++i) v[i] *= scale;
    }
    *(u32x4*)((uint16_t*)(ws + WS_KG) + (size_t)d * L_ + 8 * t8) =
        u32x4{pack2h(v[0], v[1]), pack2h(v[2], v[3]), pack2h(v[4], v[5]), pack2h(v[6], v[7])};
  }
  WAIT_VM(0);
  STAMP(8);
  STAMP_FLUSH(0);
}

constexpr int UC_S = 4160;
constexpr int UC_B4 = 4 * UC_S + 32;
constexpr int UREG = UC_B4 + 4 * UC_S;
constexpr int KOFF = UREG;
constexpr int CH_LDS = KOFF + 512 + 4096;
constexpr int K2_LDS = 2 * CH_LDS;

__global__ __launch_bounds__(256, 2) void k2_conv(const uint16_t* __restrict__ Dskip, char* __restrict__ ws) {
  extern __shared__ __attribute__((aligned(16))) char smem[];
  const int tid = threadIdx.x, lane = tid & 63;
  const int wv = __builtin_amdgcn_readfirstlane(tid >> 6);
  const int bid = blockIdx.x;
  STAMP_DECL
  const int chs = wv >> 1, half = wv & 1;
  const int d = 2 * bid + chs;
  const int tp = tid & 127;
  char* cb = smem + chs * CH_LDS;
  const unsigned lbase = (unsigned)(size_t)(lds_char*)smem + chs * CH_LDS;
  const uint16_t* uR = (const uint16_t*)(ws + WS_UR) + (size_t)d * L_;
  const uint16_t* xT = (const uint16_t*)(ws + WS_XT) + (size_t)d * L_;
  const uint16_t* Kg = (const uint16_t*)(ws + WS_KG) + (size_t)d * L_;
  const int m = lane & 15, kg = lane >> 4;
  u32x4 uu[4];
  uu[0] = *(const u32x4*)(uR + tp * 8);
  uu[1] = *(const u32x4*)(uR + tp * 8 + 8);
  uu[2] = *(const u32x4*)(uR + (tp + 128) * 8);
  uu[3] = *(const u32x4*)(uR + (tp < 127 ? tp + 129 : 255) * 8);
  glds16(Kg + (2 * half) * 512 + lane * 8, lbase + KOFF + 512 + (2 * half) * 1024);
  glds16(Kg + (2 * half + 1) * 512 + lane * 8, lbase + KOFF + 512 + (2 * half + 1) * 1024);
  u32x2 xe[4], ue[4];
#pragma unroll
  for (int j = 0; j < 4; ++j) {
    const int t0 = 256 * (4 * half + j) + 16 * m + 4 * kg;
    xe[j] = *(const u32x2*)(xT + t0);
    ue[j] = *(const u32x2*)(uR + (2044 - t0));
  }
  const float dsk = bf2f(Dskip[d]);
  const float kinv = ((const float*)(ws + WS_KS))[d];
  if (tp < 32) *(u32x4*)(cb + KOFF + tp * 16) = u32x4{0, 0, 0, 0};
  else if (tp < 40) {
    const int j = tp - 32;
    *(u32x4*)(cb + (j & 3) * UC_S + (j >> 2) * UC_B4 + 256 * 16) = u32x4{0, 0, 0, 0};
  }
  if (tp == 127) uu[3] = u32x4{0, 0, 0, 0};
#pragma unroll
  for (int it = 0; it < 2; ++it) {
    const uint32_t S[8] = {uu[2 * it][0], uu[2 * it][1], uu[2 * it][2], uu[2 * it][3],
                           uu[2 * it + 1][0], uu[2 * it + 1][1], uu[2 * it + 1][2], uu[2 * it + 1][3]};
#pragma unroll
    for (int ml = 0; ml < 8; ++ml) {
      const int s = 7 - ml;
      u32x4 o;
#pragma unroll
      for (int i = 0; i < 4; ++i)
        o[i] = (s & 1) ? __builtin_amdgcn_alignbit(S[(s >> 1) + i + 1], S[(s >> 1) + i], 16) : S[(s >> 1) + i];
      *(u32x4*)(cb + (ml & 3) * UC_S + (ml >> 2) * UC_B4 + (tp + 128 * it) * 16) = o;
    }
  }
  STAMP(1);
  WAIT_VM(0);
  __syncthreads();
  STAMP(2);

  const int ml = m & 7, mh = m >> 3;
  const char* abase = cb + (ml & 3) * UC_S + (ml >> 2) * UC_B4 + 16 * (253 - mh + kg) - 256 * half;
  const char* bbase = cb + KOFF + 512 + 32 * m + 16 * kg - 32 - 256 * half;
  f32x4 acc[8];
#pragma unroll
  for (int t = 0; t < 8; ++t) acc[t] = f32x4{0, 0, 0, 0};
  f16x8 af[2][8], bfr[2][8];
#pragma unroll
  for (int j = 0; j < 8; ++j) {
    af[0][j] = *(const f16x8*)(abase - 512 * j);
    bfr[0][j] = *(const f16x8*)(bbase + 512 * j);
  }
#pragma unroll
  for (int s = 0; s < 4; ++s) {
    if (s < 3) {
#pragma unroll
      for (int j = 0; j < 8; ++j) {
        af[(s + 1) & 1][j] = *(const f16x8*)(abase - 512 * j - 64 * (s + 1));
        bfr[(s + 1) & 1][j] = *(const f16x8*)(bbase + 512 * j - 64 * (s + 1));
      }
    }
#pragma unroll
    for (int T = 0; T < 8; ++T)
#pragma unroll
      for (int al = 0; al <= T; ++al)
        acc[T] = __builtin_amdgcn_mfma_f32_16x16x32_f16(af[s & 1][al], bfr[s & 1][T - al], acc[T], 0, 0, 0);
  }
  STAMP(3);
  __syncthreads();
  STAMP(4);
  f32x4 keep[4];
  if (half) {
#pragma unroll
    for (int j = 0; j < 4; ++j) { *(f32x4*)(cb + 4096 + j * 1024 + lane * 16) = acc[j]; keep[j] = acc[4 + j]; }
    asm volatile("" ::: "memory");
  } else {
#pragma unroll
    for (int j = 0; j < 4; ++j) { *(f32x4*)(cb + j * 1024 + lane * 16) = acc[4 + j]; keep[j] = acc[j]; }
    asm volatile("" ::: "memory");
  }
  __syncthreads();
  STAMP(5);
  uint16_t* hT = (uint16_t*)(ws + WS_HT) + (size_t)d * L_;
#pragma unroll
  for (int j = 0; j < 4; ++j) {
    const f32x4 o = *(const f32x4*)(cb + (1 - half) * 4096 + j * 1024 + lane * 16);
    const f32x4 a = keep[j];
    float hv[4];
#pragma unroll
    for (int r = 0; r < 4; ++r) {
      const float y = rbf((a[r] + o[r]) * kinv);
      const uint32_t uw = ue[j][1 - (r >> 1)];
      const f16x2 u2 = __builtin_bit_cast(f16x2, uw);
      const float uf = (float)u2[1 - (r & 1)];
      const uint32_t xw = xe[j][r >> 1];
      const float xf = (r & 1) ? bfhi(xw) : bflo(xw);
      hv[r] = xf + rbf(y + rbf(uf * dsk));
    }
    *(u32x2*)(hT + 256 * (4 * half + j) + 16 * m + 4 * kg) = u32x2{pack2(hv[0], hv[1]), pack2(hv[2], hv[3])};
  }
  STAMP(6);
  STAMP_FLUSH(1);
}

constexpr int G1_HS = 64 * 256;
constexpr int G1_WS = 64 * 224;
constexpr int G1_STAGE = G1_HS + G1_WS;
constexpr int G1_NS = 5;
constexpr int G1_ST = G1_NS * G1_STAGE;
constexpr int K3_LDS = G1_ST + 1024 + 2048;
#define G1_PRO 4
__device__ constexpr int g1_slot(int h) {
  return G1_PRO == 3 ? (h == 0 ? 3 : h == 1 ? 4 : h == 2 ? 5 : h == 3 ? 6 : h == 5 ? 7 : -1)
       : G1_PRO == 4 ? (h == 0 ? 4 : h == 1 ? 5 : h == 3 ? 6 : h == 5 ? 7 : -1)
                     : (h == 1 ? 5 : h == 3 ? 6 : h == 5 ? 7 : -1);
}
__device__ constexpr int g1_upto(int kt) {
  int m = G1_PRO - 1;
  for (int h = 0; h <= 2 * kt; ++h) m = g1_slot(h) > m ? g1_slot(h) : m;
  return m;
}

__device__ __forceinline__ bf16x4 tr_read(const char* p) {
  return __builtin_amdgcn_ds_read_tr16_b64_v4i16((lds_bf16x4*)p);
}
__device__ __forceinline__ bf16x8 cat8(bf16x4 a, bf16x4 b) {
  return bf16x8{a[0], a[1], a[2], a[3], b[0], b[1], b[2], b[3]};
}
__device__ __forceinline__ void swap16(uint32_t& a, uint32_t& b) {
  auto r = __builtin_amdgcn_permlane16_swap(a, b, false, false);
  a = r[0]; b = r[1];
}
#define WAIT_VM_N(n) asm volatile("s_waitcnt vmcnt(%0)" :: "i"(n) : "memory")

template <int WF, int NG>
__device__ __forceinline__ void k3_body(const uint16_t* __restrict__ w1, char* __restrict__ ws, char* smem,
                                        const int lane, const int wv, const int l0, const int f0, const int ft) {
  constexpr int NT = WF ? 3 : 4;
  constexpr int CI0 = WF ? 4 : 0;
  STAMP_DECL
  const int wl = wv & 3;
  const unsigned lbase = (unsigned)(size_t)(lds_char*)smem;
  const uint16_t* hT = (const uint16_t*)(ws + WS_HT);
  const int g = lane >> 4, q = (lane >> 2) & 3, pp = lane & 3, r15 = lane & 15;
  const int kt0 = ft >> 1;
  int hoff[2], woff[2];
#pragma unroll
  for (int i = 0; i < 2; ++i) {
    const int row = (wv + 8 * i) * 4 + (lane >> 4);
    hoff[i] = row * L_ + l0 + 8 * ((lane & 15) ^ (2 * (row & 7)));
    const int pw = (wv + 8 * i < 14) ? wv + 8 * i : wv;
    const int nn = pw * 64 + lane;
    const int r2 = nn / 14, ch = nn - r2 * 14;
    woff[i] = r2 * F_ + f0 + 8 * ch;
  }
  auto piece = [&](int kt, int i) {
    const unsigned sb = lbase + (kt % G1_NS) * G1_STAGE;
    const int ka = (kt0 + kt) & 7;
    const uint16_t* hs = hT + (size_t)ka * 64 * L_;
    const uint16_t* wsrc = w1 + (size_t)ka * 64 * F_;
    if (i == 0) glds16(hs + hoff[0], sb + wv * 1024);
    else if (i == 1) glds16(wsrc + woff[0], sb + G1_HS + wv * 1024);
    else if (i == 2) glds16(hs + hoff[1], sb + (wv + 8) * 1024);
    else if (NG == 4) glds16(wsrc + woff[1], sb + G1_HS + (wv + 8) * 1024);
  };
#pragma unroll
  for (int s = 0; s < G1_PRO; ++s) {
#pragma unroll
    for (int i = 0; i < 4; ++i) piece(s, i);
    if (s < G1_PRO - 1) RAW_BARRIER();
  }

  int tix[NT];
#pragma unroll
  for (int i = 0; i < NT; ++i) {
    int t = i + (WF ? (wl == 3 ? 0 : wl) : wl);
    t = t >= NT ? t - NT : t;
    tix[i] = CI0 + t;
  }
  const int xv = 2 * (4 * (g & 1) + q);
  unsigned hb[3][2], wb[3][NT];
#pragma unroll
  for (int r = 0; r < 3; ++r) {
#pragma unroll
    for (int cc = 0; cc < 2; ++cc) {
      hb[r][cc] = lbase + r * 2 * G1_STAGE + (4 * g + q) * 256 + ((((2 * (2 * wl + cc)) ^ xv) + (pp >> 1)) * 16) + 8 * (pp & 1);
      asm volatile("" : "+v"(hb[r][cc]));
    }
#pragma unroll
    for (int i = 0; i < NT; ++i) {
      wb[r][i] = lbase + r * 2 * G1_STAGE + G1_HS + (4 * g + q) * 224 + 8 * pp + 32 * tix[i];
      asm volatile("" : "+v"(wb[r][i]));
    }
  }
  const int lrow = l0 + 32 * wl + r15;

  f32x4 acc[NT][2], accS[2], accG[2];
#pragma unroll
  for (int i = 0; i < NT; ++i) { acc[i][0] = f32x4{0, 0, 0, 0}; acc[i][1] = f32x4{0, 0, 0, 0}; }
  accS[0] = accS[1] = accG[0] = accG[1] = f32x4{0, 0, 0, 0};
  bf16x8 ones = {0x3f80, 0x3f80, 0x3f80, 0x3f80, 0x3f80, 0x3f80, 0x3f80, 0x3f80};
  asm volatile("" : "+v"(ones));
  float csum[2] = {0.f, 0.f};

  struct Frag { bf16x8 h[2], w[NT]; };
  auto trl = [&](unsigned a) { return __builtin_amdgcn_ds_read_tr16_b64_v4i16((lds_bf16x4*)(size_t)a); };
  auto ld1 = [&](Frag& F, int kt, int ks, int i) {
    const int bf = kt % G1_NS, r = bf >> 1, o = (bf & 1) * G1_STAGE;
    if (i < 2) F.h[i] = cat8(trl(hb[r][i] + o + ks * 8192), trl(hb[r][i] + o + ks * 8192 + 4096));
    else F.w[i - 2] = cat8(trl(wb[r][i - 2] + o + ks * 32 * 224), trl(wb[r][i - 2] + o + (ks * 32 + 16) * 224));
  };
  constexpr int NM = 2 * NT + (WF ? 4 : 0);
  auto mm1 = [&](const Frag& F, int i) {
    if (i < 2 * NT) {
      const int ci = i >> 1, cc = i & 1;
      acc[ci][cc] = __builtin_amdgcn_mfma_f32_16x16x32_bf16(F.w[ci], F.h[cc], acc[ci][cc], 0, 0, 0);
    } else {
      const int cc = i & 1;
      if (i < 2 * NT + 2) accS[cc] = __builtin_amdgcn_mfma_f32_16x16x32_bf16(ones, F.h[cc], accS[cc], 0, 0, 0);
      else accG[cc] = __builtin_amdgcn_mfma_f32_16x16x32_bf16(F.h[cc], F.h[cc], accG[cc], 0, 0, 0);
    }
  };
  auto step = [&](const Frag& C, Frag& N, int nkt, int nks, int kstep, int iss) {
#pragma unroll
    for (int i = 0; i < NM; ++i) {
      mm1(C, i);
      __builtin_amdgcn_sched_barrier(0);
      if (iss >= 0 && i < 4) { piece(iss, i); __builtin_amdgcn_sched_barrier(0); }
      if (i < 2) {
#pragma unroll
        for (int j = 2 * i; j < 2 * i + 2; ++j) {
          const bf16v2 pr = __builtin_bit_cast(bf16v2, bf16x2_t{C.w[0][2 * j], C.w[0][2 * j + 1]});
          csum[j & 1] = __builtin_amdgcn_fdot2_f32_bf16(pr, bf16v2{(__bf16)1.0f, (__bf16)1.0f}, csum[j & 1], false);
        }
      }
      if (nkt >= 0 && i < NT + 2) ld1(N, nkt, nks, i);
      __builtin_amdgcn_sched_barrier(0);
    }
    if (!WF && kstep < 2) {
      if (kstep == (ft & 1)) {
        asm volatile("" ::: "memory");
#pragma unroll
        for (int cc = 0; cc < 2; ++cc) {
          const u32x4 hv = __builtin_bit_cast(u32x4, C.h[cc]);
          uint32_t a0 = hv[0], a1 = hv[1], b0 = hv[2], b1 = hv[3];
          swap16(a0, b0); swap16(a1, b1);
          uint16_t* p = (uint16_t*)(ws + WS_HR) + (size_t)(lrow + 16 * cc) * D_ + 32 * ft + 16 * (g & 1) + 8 * (g >> 1);
          *(u32x4*)p = u32x4{a0, a1, b0, b1};
        }
      }
      __builtin_amdgcn_sched_barrier(0);
    }
  };
  Frag F0, F1;
  STAMP(1);
  WAIT_VM_N((G1_PRO - 1) * NG);
  RAW_BARRIER();
  STAMP(2);
#pragma unroll
  for (int i = 0; i < NT + 2; ++i) ld1(F0, 0, 0, i);
#pragma unroll
  for (int kt = 0; kt < 8; ++kt) {
    STAMP(3 + kt);
    step(F0, F1, kt, 1, 2 * kt, g1_slot(2 * kt));
    if (kt < 7) {
      WAIT_VM_N((g1_upto(kt) - kt - 1) * NG);
      if (kt < 3) WAIT_LGKM0();
      RAW_BARRIER();
    }
    step(F1, F0, kt < 7 ? kt + 1 : -1, 0, 2 * kt + 1, g1_slot(2 * kt + 1));
  }
  STAMP(11);
  float* stt = (float*)(smem + G1_ST);
  float mm[2], rs[2];
  if (WF) {
#pragma unroll
    for (int cc = 0; cc < 2; ++cc) {
      const float s = accS[cc][0];
      float dg = (pp == 0) ? accG[cc][0] : (pp == 1) ? accG[cc][1] : (pp == 2) ? accG[cc][2] : accG[cc][3];
      const float sq = __shfl(dg, r15 + 16 * (r15 >> 2));
      const float mean = s * (1.0f / D_);
      const float var = fmaxf(sq * (1.0f / D_) - mean * mean, 0.f);
      mm[cc] = rbf(mean);
      const float sd = rbf(sqrtf(rbf(rbf(var) + 1e-5f)));
      rs[cc] = 1.0f / sd;
      if (g == 0) *(f32x2*)(stt + 2 * (32 * wl + 16 * cc + r15)) = f32x2{mm[cc], rs[cc]};
    }
  }
  {
    float t = csum[0] + csum[1];
    { const auto r = __builtin_amdgcn_permlane16_swap(__float_as_uint(t), __float_as_uint(t), false, false);
      t = __uint_as_float(r[0]) + __uint_as_float(r[1]); }
    { const auto r = __builtin_amdgcn_permlane32_swap(__float_as_uint(t), __float_as_uint(t), false, false);
      t = __uint_as_float(r[0]) + __uint_as_float(r[1]); }
    if (g == 0 && (!WF || wl < 3)) stt[256 + 16 * tix[0] + r15] = t;
  }
  __syncthreads();
  __builtin_amdgcn_sched_barrier(0);
  f32x4 c4[NT];
#pragma unroll
  for (int ci = 0; ci < NT; ++ci) c4[ci] = *(const f32x4*)(stt + 256 + 16 * tix[ci] + 4 * g);
  if (!WF) {
#pragma unroll
    for (int cc = 0; cc < 2; ++cc) {
      const f32x2 t = *(const f32x2*)(stt + 2 * (32 * wl + 16 * cc + r15));
      mm[cc] = t[0]; rs[cc] = t[1];
    }
  }
  uint16_t* H1 = (uint16_t*)(ws + WS_H1);
  uint32_t pk[NT][2][2];
#pragma unroll
  for (int cc = 0; cc < 2; ++cc)
#pragma unroll
    for (int ci = 0; ci < NT; ++ci)
#pragma unroll
      for (int j2 = 0; j2 < 2; ++j2) {
        const f32x2 a = {acc[ci][cc][2 * j2], acc[ci][cc][2 * j2 + 1]};
        const f32x2 c = {c4[ci][2 * j2], c4[ci][2 * j2 + 1]};
        const uint32_t vb = cvt2((a - mm[cc] * c) * rs[cc]);
        const f32x2 v = {bflo(vb), bfhi(vb)};
        const f32x2 e = v * -1.4426950408889634f;
        const f32x2 dn = f32x2{__builtin_amdgcn_exp2f(e[0]), __builtin_amdgcn_exp2f(e[1])} + 1.0f;
        const f32x2 o = v * f32x2{__builtin_amdgcn_rcpf(dn[0]), __builtin_amdgcn_rcpf(dn[1])};
        pk[ci][cc][j2] = cvt2(o);
      }
  STAMP(12);
#pragma unroll
  for (int cc = 0; cc < 2; ++cc)
#pragma unroll
    for (int cp = 0; cp < NT / 2; ++cp) {
      swap16(pk[2 * cp][cc][0], pk[2 * cp + 1][cc][0]);
      swap16(pk[2 * cp][cc][1], pk[2 * cp + 1][cc][1]);
      const int tt = tix[2 * cp] + (g & 1) * (tix[2 * cp + 1] - tix[2 * cp]);
      uint16_t* p = H1 + (size_t)(lrow + 16 * cc) * F_ + f0 + 16 * tt + 8 * (g >> 1);
      *(u32x4*)p = u32x4{pk[2 * cp][cc][0], pk[2 * cp][cc][1], pk[2 * cp + 1][cc][0], pk[2 * cp + 1][cc][1]};
    }
  if (WF) {
    swap16(pk[NT - 1][0][0], pk[NT - 1][1][0]);
    swap16(pk[NT - 1][0][1], pk[NT - 1][1][1]);
    uint16_t* p = H1 + (size_t)(lrow + 16 * (g & 1)) * F_ + f0 + 16 * tix[NT - 1] + 8 * (g >> 1);
    *(u32x4*)p = u32x4{pk[NT - 1][0][0], pk[NT - 1][0][1], pk[NT - 1][1][0], pk[NT - 1][1][1]};
  }
  STAMP(13);
  STAMP_FLUSH(2);
}

__global__ __launch_bounds__(512) void k3_gemm1(const uint16_t* __restrict__ w1, char* __restrict__ ws) {
  extern __shared__ __attribute__((aligned(16))) char smem[];
  const int tid = threadIdx.x, lane = tid & 63;
  const int wv = __builtin_amdgcn_readfirstlane(tid >> 6);
  const int bid = blockIdx.x;
  const int xcd = bid & 7, idx = bid >> 3;
  const int lt = 4 * (xcd >> 1) + (idx & 3), ft = 8 * (xcd & 1) + (idx >> 2);
  if (wv < 4) k3_body<0, 4>(w1, ws, smem, lane, wv, lt * 128, ft * 112, ft);
  else if (wv < 6) k3_body<1, 4>(w1, ws, smem, lane, wv, lt * 128, ft * 112, ft);
  else k3_body<1, 3>(w1, ws, smem, lane, wv, lt * 128, ft * 112, ft);
}

constexpr int G2_HS = 64 * 256;
constexpr int G2_WS = 128 * 128;
constexpr int G2_STAGE = G2_HS + G2_WS;
constexpr int G2_NS = 5;
constexpr int G2_NK = F_ / 128;
constexpr int K4_LDS = G2_NS * G2_STAGE;

__global__ __launch_bounds__(512) void k4_gemm2(const uint16_t* __restrict__ w2, char* __restrict__ ws,
                                                uint16_t* __restrict__ out) {
  extern __shared__ __attribute__((aligned(16))) char smem[];
  const int tid = threadIdx.x, lane = tid & 63;
  const int wv = __builtin_amdgcn_readfirstlane(tid >> 6);
  const int bid = blockIdx.x;
  const int xcd = bid & 7, idx = bid >> 3;
  STAMP_DECL
  const int lt = 4 * xcd + (idx & 3), dtile = idx >> 2;
  const int l0 = lt * 64, d0 = dtile * 64;
  const unsigned lbase = (unsigned)(size_t)(lds_char*)smem;
  const uint16_t* H1 = (const uint16_t*)(ws + WS_H1);
  const int g = lane >> 4, q = (lane >> 2) & 3, pp = lane & 3;
  const int wl = wv & 3, wd = wv >> 2;
  const int r15 = lane & 15;
  int hoff[2], woff[2];
#pragma unroll
  for (int i = 0; i < 2; ++i) {
    const int row = (wv * 2 + i) * 4 + (lane >> 4);
    hoff[i] = (l0 + row) * F_ + 8 * ((lane & 15) ^ (row & 15));
    const int r2 = (wv * 2 + i) * 8 + (lane >> 3);
    const int gf = ((r2 >> 1) & 1) | (((r2 >> 3) & 1) << 1);
    woff[i] = r2 * D_ + d0 + 8 * ((lane & 7) ^ (2 * gf));
  }
  const size_t oo = (size_t)(l0 + 16 * wl + r15) * D_ + d0 + 32 * wd + 16 * (g & 1) + 8 * (g >> 1);
  const u32x4 hres = *(const u32x4*)((const uint16_t*)(ws + WS_HR) + oo);
  auto stage = [&](int kt) {
    const unsigned sb = lbase + (kt % G2_NS) * G2_STAGE;
#pragma unroll
    for (int i = 0; i < 2; ++i) {
      glds16(w2 + woff[i] + (size_t)kt * 128 * D_, sb + G2_HS + (wv * 2 + i) * 1024);
      glds16(H1 + hoff[i] + kt * 128, sb + (wv * 2 + i) * 1024);
    }
  };
#pragma unroll
  for (int s = 0; s < 4; ++s) { stage(s); if (s < 3) RAW_BARRIER(); }

  const int hrd = (16 * wl + r15) * 256;
  int wrd[2][2];
#pragma unroll
  for (int ci = 0; ci < 2; ++ci)
#pragma unroll
    for (int t = 0; t < 2; ++t) {
      const int row = 8 * g + 4 * t + q;
      const int gf = ((row >> 1) & 1) | (((row >> 3) & 1) << 1);
      wrd[ci][t] = G2_HS + row * 128 + (((2 * (2 * wd + ci)) ^ (2 * gf)) + (pp >> 1)) * 16 + 8 * (pp & 1);
    }
  f32x4 acc[2] = {f32x4{0, 0, 0, 0}, f32x4{0, 0, 0, 0}};

  struct Frag { bf16x8 h[2], w[2][2]; };
  auto ldfrag = [&](Frag& F, int kt, int kh) {
    const char* sb = smem + (kt % G2_NS) * G2_STAGE;
#pragma unroll
    for (int k2 = 0; k2 < 2; ++k2) {
      const int ks = 2 * kh + k2;
      F.h[k2] = *(const bf16x8*)(sb + hrd + (((4 * ks + g) ^ r15) * 16));
#pragma unroll
      for (int ci = 0; ci < 2; ++ci)
        F.w[k2][ci] = cat8(tr_read(sb + wrd[ci][0] + ks * 4096), tr_read(sb + wrd[ci][1] + ks * 4096));
    }
  };
  auto mma = [&](const Frag& F) {
#pragma unroll
    for (int k2 = 0; k2 < 2; ++k2)
#pragma unroll
      for (int ci = 0; ci < 2; ++ci)
        acc[ci] = __builtin_amdgcn_mfma_f32_16x16x32_bf16(F.w[k2][ci], F.h[k2], acc[ci], 0, 0, 0);
  };
  Frag F0, F1;
  STAMP(1);
  WAIT_VM(12);
  RAW_BARRIER();
  STAMP(2);
  ldfrag(F0, 0, 0);
#pragma unroll
  for (int kt = 0; kt < G2_NK; ++kt) {
    if ((kt & 1) == 0) STAMP(3 + kt / 2);
    ldfrag(F1, kt, 1);
    mma(F0);
    if (kt < G2_NK - 1) {
      if (kt < G2_NK - 3) WAIT_VM(8);
      else if (kt == G2_NK - 3) WAIT_VM(4);
      else WAIT_VM(0);
      RAW_BARRIER();
      if (kt + 4 < G2_NK) stage(kt + 4);
      ldfrag(F0, kt + 1, 0);
    }
    mma(F1);
  }
  STAMP(10);
  {
    uint32_t a0 = pack2(acc[0][0], acc[0][1]), a1 = pack2(acc[0][2], acc[0][3]);
    uint32_t b0 = pack2(acc[1][0], acc[1][1]), b1 = pack2(acc[1][2], acc[1][3]);
    swap16(a0, b0); swap16(a1, b1);
    const uint32_t v[4] = {a0, a1, b0, b1};
    u32x4 r;
#pragma unroll
    for (int j = 0; j < 4; ++j) r[j] = pack2(bflo(hres[j]) + bflo(v[j]), bfhi(hres[j]) + bfhi(v[j]));
    *(u32x4*)(out + oo) = r;
  }
  STAMP(11);
  STAMP_FLUSH(3);
}

extern "C" void kernel_launch(void* const* d_in, const int* in_sizes, int n_in,
                              void* d_out, int out_size, void* d_ws, size_t ws_size,
                              hipStream_t stream) {
  const uint16_t* x = (const uint16_t*)d_in[0];
  const uint16_t* Are = (const uint16_t*)d_in[1];
  const uint16_t* Aim = (const uint16_t*)d_in[2];
  const uint16_t* Bre = (const uint16_t*)d_in[3];
  const uint16_t* Bim = (const uint16_t*)d_in[4];
  const uint16_t* Cre = (const uint16_t*)d_in[5];
  const uint16_t* Cim = (const uint16_t*)d_in[6];
  const uint16_t* logdt = (const uint16_t*)d_in[7];
  const uint16_t* Dskip = (const uint16_t*)d_in[8];
  const uint16_t* w1 = (const uint16_t*)d_in[10];
  const uint16_t* w2 = (const uint16_t*)d_in[11];
  char* ws = (char*)d_ws;
  (void)hipFuncSetAttribute((const void*)k1_prep, hipFuncAttributeMaxDynamicSharedMemorySize, K1_LDS);
  (void)hipFuncSetAttribute((const void*)k2_conv, hipFuncAttributeMaxDynamicSharedMemorySize, K2_LDS);
  (void)hipFuncSetAttribute((const void*)k3_gemm1, hipFuncAttributeMaxDynamicSharedMemorySize, K3_LDS);
  (void)hipFuncSetAttribute((const void*)k4_gemm2, hipFuncAttributeMaxDynamicSharedMemorySize, K4_LDS);
  k1_prep<<<256, 512, K1_LDS, stream>>>(x, Are, Aim, Bre, Bim, Cre, Cim, logdt, w1, w2, ws);
  k2_conv<<<D_ / 2, 256, K2_LDS, stream>>>(Dskip, ws);
  k3_gemm1<<<256, 512, K3_LDS, stream>>>(w1, ws);
  k4_gemm2<<<256, 512, K4_LDS, stream>>>(w2, ws, (uint16_t*)d_out);
}
```

```cpp
#include <hip/hip_runtime.h>
#include <hip/hip_bf16.h>
#include <stdint.h>
#include <type_traits>

constexpr int L_ = 2048, D_ = 512, N_ = 128, F_ = 1792;

typedef __attribute__((ext_vector_type(8))) short bf16x8;
typedef __attribute__((ext_vector_type(4))) short bf16x4;
typedef __attribute__((ext_vector_type(2))) short bf16x2_t;
typedef __attribute__((ext_vector_type(8))) _Float16 f16x8;
typedef __attribute__((ext_vector_type(2))) _Float16 f16x2;
typedef __attribute__((ext_vector_type(4))) float f32x4;
typedef __attribute__((ext_vector_type(2))) float f32x2;
typedef __attribute__((ext_vector_type(4))) unsigned u32x4;
typedef __attribute__((ext_vector_type(2))) unsigned u32x2;
typedef __attribute__((address_space(3))) bf16x4 lds_bf16x4;
typedef __attribute__((address_space(3))) char lds_char;

__device__ __forceinline__ float bf2f(uint16_t v) { return __uint_as_float(((uint32_t)v) << 16); }
__device__ __forceinline__ float bflo(uint32_t v) { return __uint_as_float(v << 16); }
__device__ __forceinline__ float bfhi(uint32_t v) { return __uint_as_float(v & 0xffff0000u); }
__device__ __forceinline__ uint16_t f2bf(float f) {
  __hip_bfloat16 b = __float2bfloat16(f);
  return *reinterpret_cast<uint16_t*>(&b);
}
__device__ __forceinline__ float rbf(float f) { return bf2f(f2bf(f)); }
__device__ __forceinline__ uint32_t pack2(float lo, float hi) { return (uint32_t)f2bf(lo) | ((uint32_t)f2bf(hi) << 16); }
__device__ __forceinline__ uint32_t pack2h(float lo, float hi) {
  f16x2 v; v[0] = (_Float16)lo; v[1] = (_Float16)hi;
  return __builtin_bit_cast(uint32_t, v);
}

__device__ __forceinline__ void glds16(const void* g, unsigned lds_dst) {
  unsigned keep;
  asm volatile("s_mov_b32 %0, m0\n\ts_mov_b32 m0, %2\n\ts_nop 0\n\tglobal_load_lds_dwordx4 %1, off\n\ts_mov_b32 m0, %0"
               : "=&s"(keep) : "v"(g), "s"(lds_dst) : "memory");
}
#define WAIT_VM(n) asm volatile("s_waitcnt vmcnt(" #n ")" ::: "memory")
#define WAIT_LGKM0() asm volatile("s_waitcnt lgkmcnt(0)" ::: "memory")
#define RAW_BARRIER() do { asm volatile("" ::: "memory"); __builtin_amdgcn_s_barrier(); asm volatile("" ::: "memory"); } while (0)

#define STAMP_DECL
#define STAMP(i)
#define STAMP_FLUSH(k)
constexpr size_t WS_UR = 0;
constexpr size_t WS_XT = WS_UR + (size_t)D_ * L_ * 2;
constexpr size_t WS_HT = WS_XT + (size_t)D_ * L_ * 2;
constexpr size_t WS_KG = WS_HT + (size_t)D_ * L_ * 2;
constexpr size_t WS_HR = WS_KG + (size_t)D_ * L_ * 2;
constexpr size_t WS_KS = WS_HR + (size_t)D_ * L_ * 2;
constexpr size_t WS_CS = WS_KS + 4096;
constexpr size_t WS_H1 = WS_CS + 8192;

constexpr int T_RS = 528;
constexpr int T_LO = 57 * T_RS;
constexpr int CH_T = 2 * T_LO;
constexpr int K1_RES = 2 * CH_T;
constexpr int K1_LN = K1_RES + 2 * 3104;
constexpr int K1_TS = 1040;
constexpr int K1_DUMP = K1_LN + 16 * K1_TS;
constexpr int K1_LDS = K1_DUMP + 1024;

typedef __attribute__((ext_vector_type(2))) __bf16 bf16v2;
__device__ __forceinline__ uint32_t cvt2(f32x2 v) {
  const bf16v2 b = __builtin_convertvector(v, bf16v2);
  return __builtin_bit_cast(uint32_t, b);
}
__device__ __forceinline__ f32x2 cmul(f32x2 a, f32x2 b) {
  const f32x2 t = f32x2{a.x, a.x} * b;
  return __builtin_elementwise_fma(f32x2{a.y, a.y}, f32x2{-b.y, b.x}, t);
}
__device__ __forceinline__ f32x2 cexp_(float x, float y) {
  const float m = __expf(x);
  const float hi = y * 0.15915493667125702f;
  const float k = rintf(hi);
  float fr = fmaf(y, 0.15915493667125702f, -k);
  fr = fmaf(y, 6.4206382432985265e-09f, fr);
  return f32x2{m * __builtin_amdgcn_cosf(fr), m * __builtin_amdgcn_sinf(fr)};
}
__device__ __forceinline__ int k_rowbase(int a) { return a < 2 ? 8 * a : a < 4 ? 8 + 4 * a : a < 8 ? 16 + 2 * a : 24 + a; }
template <int C> __device__ __forceinline__ float dppf(float v) {
  return __builtin_bit_cast(float, __builtin_amdgcn_update_dpp(0, __builtin_bit_cast(int, v), C, 0xf, 0xf, true));
}
__device__ __forceinline__ float wave_sum(float v) {
  v += dppf<0xB1>(v); v += dppf<0x4E>(v); v += dppf<0x141>(v); v += dppf<0x140>(v);
  { const auto r = __builtin_amdgcn_permlane16_swap(__float_as_uint(v), __float_as_uint(v), false, false);
    v = __uint_as_float(r[0]) + __uint_as_float(r[1]); }
  { const auto r = __builtin_amdgcn_permlane32_swap(__float_as_uint(v), __float_as_uint(v), false, false);
    v = __uint_as_float(r[0]) + __uint_as_float(r[1]); }
  return v;
}
__device__ __forceinline__ float wave_max(float v) {
  v = fmaxf(v, dppf<0xB1>(v)); v = fmaxf(v, dppf<0x4E>(v)); v = fmaxf(v, dppf<0x141>(v)); v = fmaxf(v, dppf<0x140>(v));
  { const auto r = __builtin_amdgcn_permlane16_swap(__float_as_uint(v), __float_as_uint(v), false, false);
    v = fmaxf(__uint_as_float(r[0]), __uint_as_float(r[1])); }
  { const auto r = __builtin_amdgcn_permlane32_swap(__float_as_uint(v), __float_as_uint(v), false, false);
    v = fmaxf(__uint_as_float(r[0]), __uint_as_float(r[1])); }
  return v;
}

__global__ __launch_bounds__(512) void k1_prep(
    const uint16_t* __restrict__ x, const uint16_t* __restrict__ Are, const uint16_t* __restrict__ Aim,
    const uint16_t* __restrict__ Bre, const uint16_t* __restrict__ Bim, const uint16_t* __restrict__ Cre,
    const uint16_t* __restrict__ Cim, const uint16_t* __restrict__ logdt, const uint16_t* __restrict__ w1,
    const uint16_t* __restrict__ w2, char* __restrict__ ws) {
  extern __shared__ __attribute__((aligned(16))) char smem[];
  const int tid = threadIdx.x, lane = tid & 63;
  const int wv = __builtin_amdgcn_readfirstlane(tid >> 6);
  const int bid = blockIdx.x;
  STAMP_DECL
  const int ch = wv >> 2, wq = wv & 3, part = wq >> 1;
  const int t8 = tid & 255, n = tid & 127;
  const int d = 2 * bid + ch;
  const int l0 = ((bid & 7) * 32 + (bid >> 3)) * 8;
  char* tb = smem + ch * CH_T;
  const uint16_t pAr = Are[d * N_ + n], pAi = Aim[d * N_ + n], pBr = Bre[d * N_ + n], pBi = Bim[d * N_ + n],
                 pCr = Cre[d * N_ + n], pCi = Cim[d * N_ + n], pdt = logdt[d];
  const u32x4 xr = *(const u32x4*)(x + (size_t)(l0 + wv) * D_ + lane * 8);
  {
    const float dt = rbf(expf(bf2f(pdt)));
    const float zx = dt * bf2f(pAr);
    const float zy = dt * bf2f(pAi);
    f32x2 c;
    {
      const float br = dt * bf2f(pBr), bi = dt * bf2f(pBi);
      const float cr = bf2f(pCr), ci = bf2f(pCi);
      c = f32x2{cr * br - ci * bi, -(cr * bi + ci * br)};
    }
    auto putn = [&](auto NV, const int* rows, const f32x2* v) {
      constexpr int nv = decltype(NV)::value;
      uint32_t H[nv], Lw[nv];
      f32x2 hf[nv];
#pragma unroll
      for (int i = 0; i < nv; ++i) H[i] = cvt2(v[i]);
#pragma unroll
      for (int i = 0; i < nv; ++i) hf[i] = f32x2{bflo(H[i]), bfhi(H[i])};
#pragma unroll
      for (int i = 0; i < nv; ++i) hf[i] = v[i] - hf[i];
#pragma unroll
      for (int i = 0; i < nv; ++i) Lw[i] = cvt2(hf[i]);
#pragma unroll
      for (int i = 0; i < nv; ++i) {
        *(uint32_t*)(tb + rows[i] * T_RS + 4 * n) = H[i];
        *(uint32_t*)(tb + T_LO + rows[i] * T_RS + 4 * n) = Lw[i];
      }
    };
    auto cmuln = [&](auto NV, f32x2* o, const f32x2* v, f32x2 m) {
      constexpr int nv = decltype(NV)::value;
      const f32x2 mn = f32x2{-m.y, m.x};
      f32x2 t[nv];
#pragma unroll
      for (int i = 0; i < nv; ++i) t[i] = f32x2{v[i].x, v[i].x} * m;
#pragma unroll
      for (int i = 0; i < nv; ++i) o[i] = __builtin_elementwise_fma(f32x2{v[i].y, v[i].y}, mn, t[i]);
    };
    using I4 = std::integral_constant<int, 4>;
    using I8 = std::integral_constant<int, 8>;
    using I6 = std::integral_constant<int, 6>;
    using I3 = std::integral_constant<int, 3>;
    STAMP(1);
    const f32x2 g1 = cexp_(128.f * zx, -128.f * zy);
    if (part == 0) {
      const f32x2 e1 = cexp_(zx, -zy);
      const f32x2 e8 = cexp_(8.f * zx, 8.f * zy);
      const f32x2 e2 = cmul(e1, e1), e16 = cmul(e8, e8);
      const f32x2 e4 = cmul(e2, e2), e32 = cmul(e16, e16);
      f32x2 A[8], Bv[4];
      A[0] = c; A[1] = cmul(c, e1); A[2] = cmul(c, e2);
      Bv[0] = f32x2{1.f, 0.f}; Bv[1] = e8; Bv[2] = e16; Bv[3] = cmul(e16, e8);
      A[3] = cmul(A[1], e2);
      cmuln(I4{}, A + 4, A, e4);
      { const int r[4] = {41, 42, 43, 44}; putn(I4{}, r, Bv); }
      cmuln(I4{}, Bv, Bv, e32);
      { const int r[8] = {0, 1, 2, 3, 4, 5, 6, 7}; putn(I8{}, r, A); }
      cmuln(I8{}, A, A, g1);
      { const int r[4] = {45, 46, 47, 48}; putn(I4{}, r, Bv); }
      cmuln(I4{}, Bv, Bv, e32);
      { const int r[8] = {8, 9, 10, 11, 12, 13, 14, 15}; putn(I8{}, r, A); }
      { const int r[4] = {49, 50, 51, 52}; putn(I4{}, r, Bv); }
      cmuln(I4{}, Bv, Bv, e32);
      { const int r[4] = {53, 54, 55, 56}; putn(I4{}, r, Bv); }
    } else {
      const f32x2 e2 = cexp_(2.f * zx, -2.f * zy);
      const f32x2 g2 = cmul(g1, g1), e4 = cmul(e2, e2);
      const f32x2 g4 = cmul(g2, g2);
      f32x2 Q[8], R[6], S[4];
      Q[0] = cmul(c, g2);
      Q[1] = cmul(Q[0], g1); Q[2] = cmul(Q[0], g2);
      Q[3] = cmul(Q[2], g1);
      cmuln(I4{}, Q + 4, Q, g4);
      cmuln(I6{}, R, Q, e4);
      { f32x2 t[4] = {Q[0], R[0], Q[1], R[1]}; cmuln(I4{}, S, t, e2); }
      { const int r[8] = {16, 20, 24, 26, 28, 30, 32, 33}; putn(I8{}, r, Q); }
      cmuln(I4{}, Q, Q + 4, g4);
      { const int r[6] = {18, 22, 25, 27, 29, 31}; putn(I6{}, r, R); }
      cmuln(I3{}, Q + 4, Q, g4);
      { const int r[4] = {17, 19, 21, 23}; putn(I4{}, r, S); }
      { const int r[4] = {34, 35, 36, 37}; putn(I4{}, r, Q); }
      { const int r[3] = {38, 39, 40}; putn(I3{}, r, Q + 4); }
    }
  }
  STAMP(2);
  {
    char* ln = smem + K1_LN;
    *(u32x4*)(ln + (8 + wv) * K1_TS + lane * 16) = xr;
    f32x2 v[4];
#pragma unroll
    for (int j = 0; j < 4; ++j) v[j] = f32x2{bflo(xr[j]), bfhi(xr[j])};
    const f32x2 s2 = (v[0] + v[1]) + (v[2] + v[3]);
    const float mean = wave_sum(s2.x + s2.y) * (1.0f / D_);
    f32x2 q2 = f32x2{0.f, 0.f};
#pragma unroll
    for (int j = 0; j < 4; ++j) { const f32x2 dd = v[j] - mean; q2 = __builtin_elementwise_fma(dd, dd, q2); }
    const float var = wave_sum(q2.x + q2.y) * (1.0f / D_);
    const float m = rbf(mean);
    const float sd = rbf(sqrtf(rbf(rbf(var) + 1e-5f)));
    const float r = 1.0f / sd;
    u32x4 uo;
#pragma unroll
    for (int j = 0; j < 4; ++j) {
      const uint32_t a = cvt2(v[j] - m);
      const uint32_t u = cvt2(f32x2{bflo(a), bfhi(a)} * r);
      uo[j] = pack2h(bflo(u), bfhi(u));
    }
    *(u32x4*)(ln + wv * K1_TS + lane * 16) = uo;
  }
  {
    const int gi = min(bid * 512 + tid, D_ * F_ / 8 - 1);
    const unsigned dump = (unsigned)(size_t)(lds_char*)smem + K1_DUMP;
    glds16(w1 + (size_t)gi * 8, dump);
    glds16(w2 + (size_t)gi * 8, dump);
  }
  STAMP(3);
  __syncthreads();
  STAMP(4);
  {
    const char* ln = smem + K1_LN + ch * 8 * K1_TS + t8 * 4;
    uint32_t w[8];
#pragma unroll
    for (int r = 0; r < 8; ++r) w[r] = *(const uint32_t*)(ln + (ch == 0 ? 7 - r : r) * K1_TS);
    u32x4 a0, b0;
#pragma unroll
    for (int j = 0; j < 4; ++j) {
      a0[j] = __builtin_amdgcn_perm(w[2 * j + 1], w[2 * j], 0x05040100u);
      b0[j] = __builtin_amdgcn_perm(w[2 * j + 1], w[2 * j], 0x07060302u);
    }
    uint16_t* p0 = (uint16_t*)(ws + (ch == 0 ? WS_UR : WS_XT)) + (size_t)(2 * t8) * L_ + (ch == 0 ? (L_ - 8 - l0) : l0);
    *(u32x4*)(p0) = a0;
    *(u32x4*)(p0 + L_) = b0;
  }
  STAMP(5);
  float* Kf = (float*)(smem + K1_RES + ch * 3104);
  if (wq < 3) {
    const int r16 = lane & 15, kg = lane >> 4;
    const char* ta = tb + (16 * wq + r16) * T_RS + kg * 16;
    const char* tq = tb + (41 + r16) * T_RS + kg * 16;
    f32x4 a1 = f32x4{0, 0, 0, 0}, a2 = f32x4{0, 0, 0, 0};
    bf16x8 Ah[8], Al[8], Bh[8], Bl[8];
#pragma unroll
    for (int s = 0; s < 8; ++s) {
      Ah[s] = *(const bf16x8*)(ta + 64 * s); Bh[s] = *(const bf16x8*)(tq + 64 * s);
      Bl[s] = *(const bf16x8*)(tq + T_LO + 64 * s); Al[s] = *(const bf16x8*)(ta + T_LO + 64 * s);
    }
    __builtin_amdgcn_sched_barrier(0);
#pragma unroll
    for (int s = 0; s < 8; ++s) {
      a1 = __builtin_amdgcn_mfma_f32_16x16x32_bf16(Ah[s], Bh[s], a1, 0, 0, 0);
      a2 = __builtin_amdgcn_mfma_f32_16x16x32_bf16(Ah[s], Bl[s], a2, 0, 0, 0);
      a2 = __builtin_amdgcn_mfma_f32_16x16x32_bf16(Al[s], Bh[s], a2, 0, 0, 0);
    }
    a1 += a2;
    float mx = 0.f;
#pragma unroll
    for (int j = 0; j < 4; ++j) {
      const int ix = (16 * wq + 4 * kg + j) * 16 + r16;
      Kf[ix] = a1[j];
      if (ix <= 640) mx = fmaxf(mx, fabsf(a1[j]));
    }
    mx = wave_max(mx);
    if (lane == 0) Kf[768 + wq] = mx;
  }
  STAMP(6);
  __syncthreads();
  STAMP(7);
  {
    const int a = t8 >> 4, b = t8 & 15;
    const float* kp = Kf + k_rowbase(a) * 16 + b;
    float kv[8];
#pragma unroll
    for (int i = 0; i < 8; ++i) kv[i] = kp[16 * i];
    float vn = Kf[k_rowbase((t8 + 1) >> 4) * 16 + ((t8 + 1) & 15)];
    const f32x4 m4 = *(const f32x4*)(Kf + 768);
    __builtin_amdgcn_sched_barrier(0);
    const float mx = fmaxf(fmaxf(m4[0], m4[1]), m4[2]);
    const int ex = (int)((__float_as_uint(mx) >> 23) & 255u) - 127;
    int sh = 14 - ex;
    sh = sh > 126 ? 126 : (sh < -126 ? -126 : sh);
    const float scale = __uint_as_float((uint32_t)(127 + sh) << 23);
    if (t8 == 0) ((float*)(ws + WS_KS))[d] = __uint_as_float((uint32_t)(127 - sh) << 23);
    float v[8];
    {
      const bool c2 = a < 2, c4 = a < 4, c8 = a < 8;
      const float w0 = kv[0], w1_ = kv[1], w2_ = kv[2], w3 = kv[3];
      v[0] = w0;
      v[1] = c2 ? w1_ : w0;
      v[2] = c2 ? w2_ : c4 ? w1_ : w0;
      v[3] = c2 ? w3 : c4 ? w2_ : c8 ? w1_ : w0;
      v[4] = c2 ? kv[4] : c4 ? w2_ : c8 ? w1_ : ((t8 & 1) ? vn : w0);
      v[5] = c2 ? kv[5] : c4 ? w2_ : c8 ? w1_ : vn;
      v[6] = c2 ? kv[6] : c4 ? w3 : vn;
      v[7] = c2 ? kv[7] : vn;
#pragma unroll
      for (int i = 0; i < 8; ++i) v[i] *= scale;
    }
    *(u32x4*)((uint16_t*)(ws + WS_KG) + (size_t)d * L_ + 8 * t8) =
        u32x4{pack2h(v[0], v[1]), pack2h(v[2], v[3]), pack2h(v[4], v[5]), pack2h(v[6], v[7])};
  }
  WAIT_VM(0);
  STAMP(8);
  STAMP_FLUSH(0);
}

constexpr int UC_S = 4160;
constexpr int UC_B4 = 4 * UC_S + 32;
constexpr int UREG = UC_B4 + 4 * UC_S;
constexpr int KOFF = UREG;
constexpr int CH_LDS = KOFF + 512 + 4096;
constexpr int K2_LDS = 2 * CH_LDS + 1024;

__global__ __launch_bounds__(320, 2) void k2_conv(const uint16_t* __restrict__ Dskip, const uint16_t* __restrict__ w1,
                                                  char* __restrict__ ws) {
  extern __shared__ __attribute__((aligned(16))) char smem[];
  const int tid = threadIdx.x, lane = tid & 63;
  const int wv = __builtin_amdgcn_readfirstlane(tid >> 6);
  const int bid = blockIdx.x;
  if (wv == 4) {
    RAW_BARRIER();
    const char* src = (const char*)w1 + (size_t)(bid >> 3) * 16 * (F_ * 2) + (bid & 1) * F_;
    const unsigned dump = (unsigned)(size_t)(lds_char*)smem + 2 * CH_LDS;
#pragma unroll
    for (int j = 0; j < 28; ++j) {
      const unsigned c = j * 64 + lane, r = c / 112u;
      glds16(src + r * (F_ * 2) + (c - r * 112u) * 16u, dump);
    }
    WAIT_VM(0);
    return;
  }
  STAMP_DECL
  const int chs = wv >> 1, half = wv & 1;
  const int d = 2 * bid + chs;
  const int tp = tid & 127;
  char* cb = smem + chs * CH_LDS;
  const unsigned lbase = (unsigned)(size_t)(lds_char*)smem + chs * CH_LDS;
  const uint16_t* uR = (const uint16_t*)(ws + WS_UR) + (size_t)d * L_;
  const uint16_t* xT = (const uint16_t*)(ws + WS_XT) + (size_t)d * L_;
  const uint16_t* Kg = (const uint16_t*)(ws + WS_KG) + (size_t)d * L_;
  const int m = lane & 15, kg = lane >> 4;
  u32x4 uu[4];
  uu[0] = *(const u32x4*)(uR + tp * 8);
  uu[1] = *(const u32x4*)(uR + tp * 8 + 8);
  uu[2] = *(const u32x4*)(uR + (tp + 128) * 8);
  uu[3] = *(const u32x4*)(uR + (tp < 127 ? tp + 129 : 255) * 8);
  glds16(Kg + (2 * half) * 512 + lane * 8, lbase + KOFF + 512 + (2 * half) * 1024);
  glds16(Kg + (2 * half + 1) * 512 + lane * 8, lbase + KOFF + 512 + (2 * half + 1) * 1024);
  u32x2 xe[4], ue[4];
#pragma unroll
  for (int j = 0; j < 4; ++j) {
    const int t0 = 256 * (4 * half + j) + 16 * m + 4 * kg;
    xe[j] = *(const u32x2*)(xT + t0);
    ue[j] = *(const u32x2*)(uR + (2044 - t0));
  }
  const float dsk = bf2f(Dskip[d]);
  const float kinv = ((const float*)(ws + WS_KS))[d];
  if (tp < 32) *(u32x4*)(cb + KOFF + tp * 16) = u32x4{0, 0, 0, 0};
  else if (tp < 40) {
    const int j = tp - 32;
    *(u32x4*)(cb + (j & 3) * UC_S + (j >> 2) * UC_B4 + 256 * 16) = u32x4{0, 0, 0, 0};
  }
  if (tp == 127) uu[3] = u32x4{0, 0, 0, 0};
#pragma unroll
  for (int it = 0; it < 2; ++it) {
    const uint32_t S[8] = {uu[2 * it][0], uu[2 * it][1], uu[2 * it][2], uu[2 * it][3],
                           uu[2 * it + 1][0], uu[2 * it + 1][1], uu[2 * it + 1][2], uu[2 * it + 1][3]};
#pragma unroll
    for (int ml = 0; ml < 8; ++ml) {
      const int s = 7 - ml;
      u32x4 o;
#pragma unroll
      for (int i = 0; i < 4; ++i)
        o[i] = (s & 1) ? __builtin_amdgcn_alignbit(S[(s >> 1) + i + 1], S[(s >> 1) + i], 16) : S[(s >> 1) + i];
      *(u32x4*)(cb + (ml & 3) * UC_S + (ml >> 2) * UC_B4 + (tp + 128 * it) * 16) = o;
    }
  }
  STAMP(1);
  WAIT_VM(0);
  __syncthreads();
  STAMP(2);
  const int ml = m & 7, mh = m >> 3;
  const char* abase = cb + (ml & 3) * UC_S + (ml >> 2) * UC_B4 + 16 * (253 - mh + kg) - 256 * half;
  const char* bbase = cb + KOFF + 512 + 32 * m + 16 * kg - 32 - 256 * half;
  f32x4 acc[8];
#pragma unroll
  for (int t = 0; t < 8; ++t) acc[t] = f32x4{0, 0, 0, 0};
  f16x8 af[2][8], bfr[2][8];
#pragma unroll
  for (int j = 0; j < 8; ++j) {
    af[0][j] = *(const f16x8*)(abase - 512 * j);
    bfr[0][j] = *(const f16x8*)(bbase + 512 * j);
  }
#pragma unroll
  for (int s = 0; s < 4; ++s) {
    if (s < 3) {
#pragma unroll
      for (int j = 0; j < 8; ++j) {
        af[(s + 1) & 1][j] = *(const f16x8*)(abase - 512 * j - 64 * (s + 1));
        bfr[(s + 1) & 1][j] = *(const f16x8*)(bbase + 512 * j - 64 * (s + 1));
      }
    }
#pragma unroll
    for (int T = 0; T < 8; ++T)
#pragma unroll
      for (int al = 0; al <= T; ++al)
        acc[T] = __builtin_amdgcn_mfma_f32_16x16x32_f16(af[s & 1][al], bfr[s & 1][T - al], acc[T], 0, 0, 0);
  }
  STAMP(3);
  __syncthreads();
  STAMP(4);
  f32x4 keep[4];
  if (half) {
#pragma unroll
    for (int j = 0; j < 4; ++j) { *(f32x4*)(cb + 4096 + j * 1024 + lane * 16) = acc[j]; keep[j] = acc[4 + j]; }
    asm volatile("" ::: "memory");
  } else {
#pragma unroll
    for (int j = 0; j < 4; ++j) { *(f32x4*)(cb + j * 1024 + lane * 16) = acc[4 + j]; keep[j] = acc[j]; }
    asm volatile("" ::: "memory");
  }
  __syncthreads();
  STAMP(5);
  uint16_t* hT = (uint16_t*)(ws + WS_HT) + (size_t)d * L_;
#pragma unroll
  for (int j = 0; j < 4; ++j) {
    const f32x4 o = *(const f32x4*)(cb + (1 - half) * 4096 + j * 1024 + lane * 16);
    const f32x4 a = keep[j];
    float hv[4];
#pragma unroll
    for (int r = 0; r < 4; ++r) {
      const float y = rbf((a[r] + o[r]) * kinv);
      const uint32_t uw = ue[j][1 - (r >> 1)];
      const f16x2 u2 = __builtin_bit_cast(f16x2, uw);
      const float uf = (float)u2[1 - (r & 1)];
      const uint32_t xw = xe[j][r >> 1];
      const float xf = (r & 1) ? bfhi(xw) : bflo(xw);
      hv[r] = xf + rbf(y + rbf(uf * dsk));
    }
    *(u32x2*)(hT + 256 * (4 * half + j) + 16 * m + 4 * kg) = u32x2{pack2(hv[0], hv[1]), pack2(hv[2], hv[3])};
  }
  WAIT_VM(0);
  STAMP(6);
  STAMP_FLUSH(1);
}

constexpr int G1_HS = 64 * 256;
constexpr int G1_WS = 64 * 224;
constexpr int G1_STAGE = G1_HS + G1_WS;
constexpr int G1_NS = 5;
constexpr int G1_ST = G1_NS * G1_STAGE;
constexpr int G1_DUMP = G1_ST + 2048;
constexpr int K3_LDS = G1_DUMP + 1024;
#define G1_LA 2
#define PF2_A 2
__device__ constexpr int g1_pfs(int kt) {
  return kt <= -1 ? 0 : kt <= 5 ? PF2_A * (kt + 1) : kt == 6 ? PF2_A * 6 + (28 - PF2_A * 6) / 2 : 28;
}

__device__ __forceinline__ bf16x4 tr_read(const char* p) {
  return __builtin_amdgcn_ds_read_tr16_b64_v4i16((lds_bf16x4*)p);
}
__device__ __forceinline__ bf16x8 cat8(bf16x4 a, bf16x4 b) {
  return bf16x8{a[0], a[1], a[2], a[3], b[0], b[1], b[2], b[3]};
}
__device__ __forceinline__ void swap16(uint32_t& a, uint32_t& b) {
  auto r = __builtin_amdgcn_permlane16_swap(a, b, false, false);
  a = r[0]; b = r[1];
}
__device__ __forceinline__ float sum_g(float t) {
  { const auto r = __builtin_amdgcn_permlane16_swap(__float_as_uint(t), __float_as_uint(t), false, false);
    t = __uint_as_float(r[0]) + __uint_as_float(r[1]); }
  { const auto r = __builtin_amdgcn_permlane32_swap(__float_as_uint(t), __float_as_uint(t), false, false);
    t = __uint_as_float(r[0]) + __uint_as_float(r[1]); }
  return t;
}
#define WAIT_VM_N(n) asm volatile("s_waitcnt vmcnt(%0)" :: "i"(n) : "memory")
__device__ __forceinline__ void glds16s(unsigned voff, const void* sbase, unsigned lds_dst) {
  asm volatile("s_mov_b32 m0, %2\n\ts_nop 0\n\tglobal_load_lds_dwordx4 %0, %1" :: "v"(voff), "s"(sbase), "s"(lds_dst)
               : "memory");
}

template <int WF>
__device__ __forceinline__ void k3_body(char* __restrict__ ws, char* smem,
                                        const int lane, const int wv, const int l0, const int f0, const int ft) {
  constexpr int NT = WF ? 3 : 4;
  constexpr int CI0 = WF ? 4 : 0;
  STAMP_DECL
  const int wl = wv & 3;
  const unsigned lbase = (unsigned)(size_t)(lds_char*)smem;
  const int g = lane >> 4, q = (lane >> 2) & 3, pp = lane & 3, r15 = lane & 15;
  int tix[NT];
#pragma unroll
  for (int i = 0; i < NT; ++i) {
    int t = i + (WF ? (wl == 3 ? 0 : wl) : wl);
    t = t >= NT ? t - NT : t;
    tix[i] = CI0 + t;
  }
  const int xv = 2 * (4 * (g & 1) + q);
  unsigned hb[3][2], wb[3][NT];
#pragma unroll
  for (int r = 0; r < 3; ++r) {
#pragma unroll
    for (int cc = 0; cc < 2; ++cc) {
      hb[r][cc] = lbase + r * 2 * G1_STAGE + (4 * g + q) * 256 + ((((2 * (2 * wl + cc)) ^ xv) + (pp >> 1)) * 16) + 8 * (pp & 1);
      asm volatile("" : "+v"(hb[r][cc]));
    }
#pragma unroll
    for (int i = 0; i < NT; ++i) {
      wb[r][i] = lbase + r * 2 * G1_STAGE + G1_HS + (4 * g + q) * 224 + 8 * pp + 32 * tix[i];
      asm volatile("" : "+v"(wb[r][i]));
    }
  }
  const int lrow = l0 + 32 * wl + r15;

  f32x4 acc[NT][2];
#pragma unroll
  for (int i = 0; i < NT; ++i) { acc[i][0] = f32x4{0, 0, 0, 0}; acc[i][1] = f32x4{0, 0, 0, 0}; }
  f32x4 accS[2], accG[2], accC;
  accS[0] = accS[1] = accG[0] = accG[1] = accC = f32x4{0, 0, 0, 0};
  bf16x8 ones = {0x3f80, 0x3f80, 0x3f80, 0x3f80, 0x3f80, 0x3f80, 0x3f80, 0x3f80};
  asm volatile("" : "+v"(ones));

  struct Frag { bf16x8 h[2], w[NT]; };
  auto trl = [&](unsigned a) { return __builtin_amdgcn_ds_read_tr16_b64_v4i16((lds_bf16x4*)(size_t)a); };
  auto ld1 = [&](Frag& F, int kt, int ks, int i) {
    const int bf = kt % G1_NS, r = bf >> 1, o = (bf & 1) * G1_STAGE;
    if (i < 2) F.h[i] = cat8(trl(hb[r][i] + o + ks * 8192), trl(hb[r][i] + o + ks * 8192 + 4096));
    else F.w[i - 2] = cat8(trl(wb[r][i - 2] + o + ks * 32 * 224), trl(wb[r][i - 2] + o + (ks * 32 + 16) * 224));
  };
  constexpr int NM = 2 * NT + 1 + (WF ? 4 : 0);
  auto mm1 = [&](const Frag& F, int i) {
    if (i < 2 * NT) {
      const int ci = i >> 1, cc = i & 1;
      acc[ci][cc] = __builtin_amdgcn_mfma_f32_16x16x32_bf16(F.w[ci], F.h[cc], acc[ci][cc], 0, 0, 0);
    } else if (i == 2 * NT) {
      accC = __builtin_amdgcn_mfma_f32_16x16x32_bf16(F.w[0], ones, accC, 0, 0, 0);
    } else {
      const int cc = i & 1;
      if (i < 2 * NT + 3) accS[cc] = __builtin_amdgcn_mfma_f32_16x16x32_bf16(ones, F.h[cc], accS[cc], 0, 0, 0);
      else accG[cc] = __builtin_amdgcn_mfma_f32_16x16x32_bf16(F.h[cc], F.h[cc], accG[cc], 0, 0, 0);
    }
  };
  auto step = [&](const Frag& C, Frag& N, int nkt, int nks, int kstep) {
#pragma unroll
    for (int i = 0; i < NM; ++i) {
      mm1(C, kstep == 15 ? NM - 1 - i : i);
      __builtin_amdgcn_sched_barrier(0);
      if (nkt >= 0 && i < NT + 2) ld1(N, nkt, nks, i);
      __builtin_amdgcn_sched_barrier(0);
    }
    if (!WF && kstep < 2) {
      if (kstep == (ft & 1)) {
        asm volatile("" ::: "memory");
#pragma unroll
        for (int cc = 0; cc < 2; ++cc) {
          const u32x4 hv = __builtin_bit_cast(u32x4, C.h[cc]);
          uint32_t a0 = hv[0], a1 = hv[1], b0 = hv[2], b1 = hv[3];
          swap16(a0, b0); swap16(a1, b1);
          uint16_t* p = (uint16_t*)(ws + WS_HR) + (size_t)(lrow + 16 * cc) * D_ + 32 * ft + 16 * (g & 1) + 8 * (g >> 1);
          *(u32x4*)p = u32x4{a0, a1, b0, b1};
        }
      }
      __builtin_amdgcn_sched_barrier(0);
    }
  };
  Frag F0, F1;
  STAMP(1);
  RAW_BARRIER();
  STAMP(2);
#pragma unroll
  for (int i = 0; i < NT + 2; ++i) ld1(F0, 0, 0, i);
#pragma unroll
  for (int kt = 0; kt < 8; ++kt) {
    STAMP(3 + kt);
    step(F0, F1, kt, 1, 2 * kt);
    if (kt < 7) {
      if (G1_LA == G1_NS - 1 && kt + G1_NS < 8) WAIT_LGKM0();
      RAW_BARRIER();
    }
    step(F1, F0, kt < 7 ? kt + 1 : -1, 0, 2 * kt + 1);
  }
  STAMP(11);
  float* stt = (float*)(smem + G1_ST);
  float mm[2], rs[2];
  if (r15 == 0 && (!WF || wl < 3)) *(f32x4*)(stt + 256 + 16 * tix[0] + 4 * g) = accC;
  if (WF) {
    f32x2 sq, mean;
#pragma unroll
    for (int cc = 0; cc < 2; ++cc) {
      sq[cc] = (pp == 0) ? accG[cc][0] : (pp == 1) ? accG[cc][1] : (pp == 2) ? accG[cc][2] : accG[cc][3];
      mean[cc] = accS[cc][0];
    }
    mean *= (1.0f / D_);
    f32x2 var = sq * (1.0f / D_) - mean * mean;
    var = f32x2{fmaxf(var[0], 0.f), fmaxf(var[1], 0.f)};
    const uint32_t mb = cvt2(mean);
    uint32_t t = cvt2(var);
    t = cvt2(f32x2{bflo(t), bfhi(t)} + 1e-5f);
    t = cvt2(f32x2{__builtin_amdgcn_sqrtf(bflo(t)), __builtin_amdgcn_sqrtf(bfhi(t))});
    const float r0 = __builtin_amdgcn_rcpf(bflo(t)), r1 = __builtin_amdgcn_rcpf(bfhi(t));
    if (g == q) {
      *(f32x2*)(stt + 2 * (32 * wl + r15)) = f32x2{bflo(mb), r0};
      *(f32x2*)(stt + 2 * (32 * wl + 16 + r15)) = f32x2{bfhi(mb), r1};
    }
  }
  WAIT_LGKM0();
  RAW_BARRIER();
  __builtin_amdgcn_sched_barrier(0);
  f32x4 c4[NT];
#pragma unroll
  for (int ci = 0; ci < NT; ++ci) c4[ci] = *(const f32x4*)(stt + 256 + 16 * tix[ci] + 4 * g);
#pragma unroll
  for (int cc = 0; cc < 2; ++cc) {
    const f32x2 t = *(const f32x2*)(stt + 2 * (32 * wl + 16 * cc + r15));
    mm[cc] = t[0]; rs[cc] = t[1];
  }
  STAMP(12);
  uint16_t* H1 = (uint16_t*)(ws + WS_H1);
#pragma unroll
  for (int ci = 0; ci < NT; ++ci) {
    uint32_t pk[2][2];
#pragma unroll
    for (int cc = 0; cc < 2; ++cc)
#pragma unroll
      for (int j2 = 0; j2 < 2; ++j2) {
        const f32x2 a = {acc[ci][cc][2 * j2], acc[ci][cc][2 * j2 + 1]};
        const f32x2 c = {c4[ci][2 * j2], c4[ci][2 * j2 + 1]};
        const uint32_t vb = cvt2((a - mm[cc] * c) * rs[cc]);
        const f32x2 v = {bflo(vb), bfhi(vb)};
        const f32x2 e = v * -1.4426950408889634f;
        const f32x2 dn = f32x2{__builtin_amdgcn_exp2f(e[0]), __builtin_amdgcn_exp2f(e[1])} + 1.0f;
        const f32x2 o = v * f32x2{__builtin_amdgcn_rcpf(dn[0]), __builtin_amdgcn_rcpf(dn[1])};
        pk[cc][j2] = cvt2(o);
      }
    swap16(pk[0][0], pk[1][0]);
    swap16(pk[0][1], pk[1][1]);
    uint16_t* p = H1 + (size_t)(lrow + 16 * (g & 1)) * F_ + f0 + 16 * tix[ci] + 8 * (g >> 1);
    *(u32x4*)p = u32x4{pk[0][0], pk[0][1], pk[1][0], pk[1][1]};
    __builtin_amdgcn_sched_barrier(0);
  }
  STAMP(13);
  STAMP_FLUSH(2);
}

__global__ __launch_bounds__(640) void k3_gemm1(const uint16_t* __restrict__ w1, const uint16_t* __restrict__ w2,
                                                char* __restrict__ ws) {
  extern __shared__ __attribute__((aligned(16))) char smem[];
  const int tid = threadIdx.x, lane = tid & 63;
  const int wv = __builtin_amdgcn_readfirstlane(tid >> 6);
  const int bid = blockIdx.x;
  const int xcd = bid & 7, idx = bid >> 3;
  const int lt = 4 * (xcd >> 1) + (idx & 3), ft = 8 * (xcd & 1) + (idx >> 2);
  const int l0 = lt * 128, f0 = ft * 112;
  if (wv >= 8) {
    const unsigned lbase = (unsigned)(size_t)(lds_char*)smem;
    const uint16_t* hT = (const uint16_t*)(ws + WS_HT);
    const int kt0 = ft >> 1;
    const int lw = wv - 8;
    unsigned hoff[8], woff[7];
#pragma unroll
    for (int i = 0; i < 8; ++i) {
      const int row = (8 * lw + i) * 4 + (lane >> 4);
      hoff[i] = 2u * (row * L_ + l0 + 8 * ((lane & 15) ^ (2 * (row & 7))));
    }
#pragma unroll
    for (int i = 0; i < 7; ++i) {
      const int nn = (7 * lw + i) * 64 + lane;
      const int r2 = nn / 14, ch = nn - r2 * 14;
      woff[i] = 2u * (r2 * F_ + f0 + 8 * ch);
    }
    auto stage = [&](int kt) {
      const unsigned sb = lbase + (kt % G1_NS) * G1_STAGE;
      const int ka = (kt0 + kt) & 7;
      const uint16_t* hs = hT + (size_t)ka * 64 * L_;
      const uint16_t* wsrc = w1 + (size_t)ka * 64 * F_;
#pragma unroll
      for (int i = 0; i < 8; ++i) {
        glds16s(hoff[i], hs, sb + (8 * lw + i) * 1024);
        if (i < 7) glds16s(woff[i], wsrc, sb + G1_HS + (7 * lw + i) * 1024);
      }
    };
    const char* src = (const char*)w2 + (size_t)idx * 57344 + lw * 28672;
#pragma unroll
    for (int s = 0; s < G1_LA; ++s) stage(s);
#pragma unroll
    for (int kt = -1; kt < 7; ++kt) {
      WAIT_VM_N(((kt + G1_LA < 7 ? kt + G1_LA : 7) - kt - 1) * 15 + g1_pfs(kt) - g1_pfs(kt - G1_LA));
      RAW_BARRIER();
      if (kt + 1 + G1_LA < 8) stage(kt + 1 + G1_LA);
#pragma unroll
      for (int j = g1_pfs(kt); j < g1_pfs(kt + 1); ++j) glds16s(lane * 16u + j * 1024u, src, lbase + G1_DUMP);
    }
    RAW_BARRIER();
    WAIT_VM(0);
  } else if (wv < 4) k3_body<0>(ws, smem, lane, wv, l0, f0, ft);
  else k3_body<1>(ws, smem, lane, wv, l0, f0, ft);
}

constexpr int G2_HS = 64 * 256;
constexpr int G2_WS = 128 * 128;
constexpr int G2_STAGE = G2_HS + G2_WS;
constexpr int G2_NS = 5;
constexpr int G2_NK = F_ / 128;
constexpr int K4_LDS = G2_NS * G2_STAGE;

#define NLD 4
#define G2_LA 2
__global__ __launch_bounds__(256 + 64 * NLD) void k4_gemm2(const uint16_t* __restrict__ w2, char* __restrict__ ws,
                                                uint16_t* __restrict__ out) {
  extern __shared__ __attribute__((aligned(16))) char smem[];
  const int tid = threadIdx.x, lane = tid & 63;
  const int wv = __builtin_amdgcn_readfirstlane(tid >> 6);
  const int bid = blockIdx.x;
  const int xcd = bid & 7, idx = bid >> 3;
  STAMP_DECL
  const int lt = 4 * xcd + (idx & 3), dtile = idx >> 2;
  const int l0 = lt * 64, d0 = dtile * 64;
  const unsigned lbase = (unsigned)(size_t)(lds_char*)smem;
  const uint16_t* H1 = (const uint16_t*)(ws + WS_H1);
  if (wv >= 4) {
    constexpr int PP = 16 / NLD;
    const int p0 = (wv - 4) * PP;
    unsigned hoff[PP], woff[PP];
#pragma unroll
    for (int i = 0; i < PP; ++i) {
      const int row = (p0 + i) * 4 + (lane >> 4);
      hoff[i] = 2u * ((l0 + row) * F_ + 8 * ((lane & 15) ^ (row & 15)));
      const int r2 = (p0 + i) * 8 + (lane >> 3);
      const int gf = ((r2 >> 1) & 1) | (((r2 >> 3) & 1) << 1);
      woff[i] = 2u * (r2 * D_ + d0 + 8 * ((lane & 7) ^ (2 * gf)));
    }
    auto stage = [&](int kt) {
      const unsigned sb = lbase + (kt % G2_NS) * G2_STAGE + p0 * 1024;
#pragma unroll
      for (int i = 0; i < PP; ++i) {
        glds16s(woff[i], w2 + (size_t)kt * 128 * D_, sb + G2_HS + i * 1024);
        glds16s(hoff[i], H1 + kt * 128, sb + i * 1024);
      }
    };
#pragma unroll
    for (int s = 0; s < G2_LA; ++s) stage(s);
#pragma unroll
    for (int kt = -1; kt < G2_NK - 1; ++kt) {
      WAIT_VM_N(((kt + G2_LA < G2_NK - 1 ? kt + G2_LA : G2_NK - 1) - kt - 1) * 2 * PP);
      RAW_BARRIER();
      if (kt + 1 + G2_LA < G2_NK) stage(kt + 1 + G2_LA);
    }
    return;
  }
  const int g = lane >> 4, q = (lane >> 2) & 3, pp = lane & 3;
  const int wl = wv & 1, wd = wv >> 1;
  const int r15 = lane & 15;
  const size_t oo = (size_t)(l0 + 32 * wl + r15) * D_ + d0 + 32 * wd + 16 * (g & 1) + 8 * (g >> 1);
  u32x4 hres[2];
  hres[0] = *(const u32x4*)((const uint16_t*)(ws + WS_HR) + oo);
  hres[1] = *(const u32x4*)((const uint16_t*)(ws + WS_HR) + oo + 16 * D_);

  int hrd[4];
#pragma unroll
  for (int ks = 0; ks < 4; ++ks) hrd[ks] = (32 * wl + r15) * 256 + (((4 * ks + g) ^ r15) * 16);
  int wrd[2][2];
#pragma unroll
  for (int ci = 0; ci < 2; ++ci)
#pragma unroll
    for (int t = 0; t < 2; ++t) {
      const int row = 8 * g + 4 * t + q;
      const int gf = ((row >> 1) & 1) | (((row >> 3) & 1) << 1);
      wrd[ci][t] = G2_HS + row * 128 + (((2 * (2 * wd + ci)) ^ (2 * gf)) + (pp >> 1)) * 16 + 8 * (pp & 1);
    }
  f32x4 acc[2][2];
  acc[0][0] = acc[0][1] = acc[1][0] = acc[1][1] = f32x4{0, 0, 0, 0};

  struct Frag { bf16x8 h[2], w[2]; };
  auto ldfrag = [&](Frag& F, int kt, int ks) {
    const char* sb = smem + (kt % G2_NS) * G2_STAGE;
    F.h[0] = *(const bf16x8*)(sb + hrd[ks]);
    F.w[0] = cat8(tr_read(sb + wrd[0][0] + ks * 4096), tr_read(sb + wrd[0][1] + ks * 4096));
    F.w[1] = cat8(tr_read(sb + wrd[1][0] + ks * 4096), tr_read(sb + wrd[1][1] + ks * 4096));
    F.h[1] = *(const bf16x8*)(sb + hrd[ks] + 4096);
    __builtin_amdgcn_sched_barrier(0);
  };
  auto mma = [&](const Frag& F) {
#pragma unroll
    for (int a = 0; a < 2; ++a)
#pragma unroll
      for (int ci = 0; ci < 2; ++ci)
        acc[a][ci] = __builtin_amdgcn_mfma_f32_16x16x32_bf16(F.w[ci], F.h[a], acc[a][ci], 0, 0, 0);
    __builtin_amdgcn_sched_barrier(0);
  };
  Frag F0, F1, F2, F3;
  STAMP(1);
  RAW_BARRIER();
  STAMP(2);
  ldfrag(F0, 0, 0);
  ldfrag(F1, 0, 1);
#pragma unroll
  for (int kt = 0; kt < G2_NK; ++kt) {
    if ((kt & 1) == 0) STAMP(3 + kt / 2);
    ldfrag(F2, kt, 2);
    mma(F0);
    ldfrag(F3, kt, 3);
    mma(F1);
    if (kt < G2_NK - 1) {
      if (G2_LA == 4 && kt + 5 < G2_NK) WAIT_LGKM0();
      RAW_BARRIER();
      ldfrag(F0, kt + 1, 0);
    }
    mma(F2);
    if (kt < G2_NK - 1) ldfrag(F1, kt + 1, 1);
    mma(F3);
  }
  STAMP(10);
#pragma unroll
  for (int a = 0; a < 2; ++a) {
    uint32_t a0 = pack2(acc[a][0][0], acc[a][0][1]), a1 = pack2(acc[a][0][2], acc[a][0][3]);
    uint32_t b0 = pack2(acc[a][1][0], acc[a][1][1]), b1 = pack2(acc[a][1][2], acc[a][1][3]);
    swap16(a0, b0); swap16(a1, b1);
    const uint32_t v[4] = {a0, a1, b0, b1};
    u32x4 r;
#pragma unroll
    for (int j = 0; j < 4; ++j) r[j] = pack2(bflo(hres[a][j]) + bflo(v[j]), bfhi(hres[a][j]) + bfhi(v[j]));
    *(u32x4*)(out + oo + a * 16 * D_) = r;
  }
  STAMP(11);
  STAMP_FLUSH(3);
}

extern "C" void kernel_launch(void* const* d_in, const int* in_sizes, int n_in,
                              void* d_out, int out_size, void* d_ws, size_t ws_size,
                              hipStream_t stream) {
  const uint16_t* x = (const uint16_t*)d_in[0];
  const uint16_t* Are = (const uint16_t*)d_in[1];
  const uint16_t* Aim = (const uint16_t*)d_in[2];
  const uint16_t* Bre = (const uint16_t*)d_in[3];
  const uint16_t* Bim = (const uint16_t*)d_in[4];
  const uint16_t* Cre = (const uint16_t*)d_in[5];
  const uint16_t* Cim = (const uint16_t*)d_in[6];
  const uint16_t* logdt = (const uint16_t*)d_in[7];
  const uint16_t* Dskip = (const uint16_t*)d_in[8];
  const uint16_t* w1 = (const uint16_t*)d_in[10];
  const uint16_t* w2 = (const uint16_t*)d_in[11];
  char* ws = (char*)d_ws;
  (void)hipFuncSetAttribute((const void*)k1_prep, hipFuncAttributeMaxDynamicSharedMemorySize, K1_LDS);
  (void)hipFuncSetAttribute((const void*)k2_conv, hipFuncAttributeMaxDynamicSharedMemorySize, K2_LDS);
  (void)hipFuncSetAttribute((const void*)k3_gemm1, hipFuncAttributeMaxDynamicSharedMemorySize, K3_LDS);
  (void)hipFuncSetAttribute((const void*)k4_gemm2, hipFuncAttributeMaxDynamicSharedMemorySize, K4_LDS);
  k1_prep<<<256, 512, K1_LDS, stream>>>(x, Are, Aim, Bre, Bim, Cre, Cim, logdt, w1, w2, ws);
  k2_conv<<<D_ / 2, 320, K2_LDS, stream>>>(Dskip, w1, ws);
  k3_gemm1<<<256, 640, K3_LDS, stream>>>(w1, w2, ws);
  k4_gemm2<<<256, 256 + 64 * NLD, K4_LDS, stream>>>(w2, ws, (uint16_t*)d_out);
}
```

```cpp
#include <hip/hip_runtime.h>
#include <hip/hip_bf16.h>
#include <stdint.h>
#include <type_traits>

constexpr int L_ = 2048, D_ = 512, N_ = 128, F_ = 1792;

typedef __attribute__((ext_vector_type(8))) short bf16x8;
typedef __attribute__((ext_vector_type(4))) short bf16x4;
typedef __attribute__((ext_vector_type(2))) short bf16x2_t;
typedef __attribute__((ext_vector_type(8))) _Float16 f16x8;
typedef __attribute__((ext_vector_type(2))) _Float16 f16x2;
typedef __attribute__((ext_vector_type(4))) float f32x4;
typedef __attribute__((ext_vector_type(2))) float f32x2;
typedef __attribute__((ext_vector_type(4))) unsigned u32x4;
typedef __attribute__((ext_vector_type(2))) unsigned u32x2;
typedef __attribute__((address_space(3))) bf16x4 lds_bf16x4;
typedef __attribute__((address_space(3))) char lds_char;

__device__ __forceinline__ float bf2f(uint16_t v) { return __uint_as_float(((uint32_t)v) << 16); }
__device__ __forceinline__ float bflo(uint32_t v) { return __uint_as_float(v << 16); }
__device__ __forceinline__ float bfhi(uint32_t v) { return __uint_as_float(v & 0xffff0000u); }
__device__ __forceinline__ uint16_t f2bf(float f) {
  __hip_bfloat16 b = __float2bfloat16(f);
  return *reinterpret_cast<uint16_t*>(&b);
}
__device__ __forceinline__ float rbf(float f) { return bf2f(f2bf(f)); }
__device__ __forceinline__ uint32_t pack2(float lo, float hi) { return (uint32_t)f2bf(lo) | ((uint32_t)f2bf(hi) << 16); }
__device__ __forceinline__ uint32_t pack2h(float lo, float hi) {
  f16x2 v; v[0] = (_Float16)lo; v[1] = (_Float16)hi;
  return __builtin_bit_cast(uint32_t, v);
}

__device__ __forceinline__ void glds16(const void* g, unsigned lds_dst) {
  unsigned keep;
  asm volatile("s_mov_b32 %0, m0\n\ts_mov_b32 m0, %2\n\ts_nop 0\n\tglobal_load_lds_dwordx4 %1, off\n\ts_mov_b32 m0, %0"
               : "=&s"(keep) : "v"(g), "s"(lds_dst) : "memory");
}
template <int M> __device__ __forceinline__ void st16(void* p, u32x4 v) {
  if (M == 0) *(u32x4*)p = v;
  else if (M == 1) asm volatile("global_store_dwordx4 %0, %1, off nt" :: "v"(p), "v"(v) : "memory");
  else if (M == 2) asm volatile("global_store_dwordx4 %0, %1, off sc1" :: "v"(p), "v"(v) : "memory");
  else if (M == 3) asm volatile("global_store_dwordx4 %0, %1, off sc0 sc1" :: "v"(p), "v"(v) : "memory");
  else asm volatile("global_store_dwordx4 %0, %1, off sc1 nt" :: "v"(p), "v"(v) : "memory");
}
#define SM1 0
#define SM1K 0
#define SM3 0
#define WAIT_VM(n) asm volatile("s_waitcnt vmcnt(" #n ")" ::: "memory")
#define WAIT_LGKM0() asm volatile("s_waitcnt lgkmcnt(0)" ::: "memory")
#define RAW_BARRIER() do { asm volatile("" ::: "memory"); __builtin_amdgcn_s_barrier(); asm volatile("" ::: "memory"); } while (0)

#define STAMP_DECL
#define STAMP(i)
#define STAMP_FLUSH(k)
constexpr size_t WS_UR = 0;
constexpr size_t WS_XT = WS_UR + (size_t)D_ * L_ * 2;
constexpr size_t WS_HT = WS_XT + (size_t)D_ * L_ * 2;
constexpr size_t WS_KG = WS_HT + (size_t)D_ * L_ * 2;
constexpr size_t WS_HR = WS_KG + (size_t)D_ * L_ * 2;
constexpr size_t WS_KS = WS_HR + (size_t)D_ * L_ * 2;
constexpr size_t WS_CS = WS_KS + 4096;
constexpr size_t WS_H1 = WS_CS + 8192;

constexpr int K1_TS = 1040;
constexpr int K1_DUMP = 16 * K1_TS;
constexpr int K1_LDS = K1_DUMP + 1024;

typedef __attribute__((ext_vector_type(2))) __bf16 bf16v2;
__device__ __forceinline__ uint32_t cvt2(f32x2 v) {
  const bf16v2 b = __builtin_convertvector(v, bf16v2);
  return __builtin_bit_cast(uint32_t, b);
}
__device__ __forceinline__ uint32_t cvt2h(f32x2 v) {
  const f16x2 b = __builtin_convertvector(v, f16x2);
  return __builtin_bit_cast(uint32_t, b);
}
__device__ __forceinline__ f32x2 cmul(f32x2 a, f32x2 b) {
  const f32x2 t = f32x2{a.x, a.x} * b;
  return __builtin_elementwise_fma(f32x2{a.y, a.y}, f32x2{-b.y, b.x}, t);
}
__device__ __forceinline__ f32x2 cexp_(float x, float y) {
  const float m = __expf(x);
  const float hi = y * 0.15915493667125702f;
  const float k = rintf(hi);
  float fr = fmaf(y, 0.15915493667125702f, -k);
  fr = fmaf(y, 6.4206382432985265e-09f, fr);
  return f32x2{m * __builtin_amdgcn_cosf(fr), m * __builtin_amdgcn_sinf(fr)};
}
__device__ __forceinline__ int k_rowbase(int a) { return a < 2 ? 8 * a : a < 4 ? 8 + 4 * a : a < 8 ? 16 + 2 * a : 24 + a; }
template <int C> __device__ __forceinline__ float dppf(float v) {
  return __builtin_bit_cast(float, __builtin_amdgcn_update_dpp(0, __builtin_bit_cast(int, v), C, 0xf, 0xf, true));
}
__device__ __forceinline__ float wave_sum(float v) {
  v += dppf<0xB1>(v); v += dppf<0x4E>(v); v += dppf<0x141>(v); v += dppf<0x140>(v);
  { const auto r = __builtin_amdgcn_permlane16_swap(__float_as_uint(v), __float_as_uint(v), false, false);
    v = __uint_as_float(r[0]) + __uint_as_float(r[1]); }
  { const auto r = __builtin_amdgcn_permlane32_swap(__float_as_uint(v), __float_as_uint(v), false, false);
    v = __uint_as_float(r[0]) + __uint_as_float(r[1]); }
  return v;
}
__device__ __forceinline__ float wave_max(float v) {
  v = fmaxf(v, dppf<0xB1>(v)); v = fmaxf(v, dppf<0x4E>(v)); v = fmaxf(v, dppf<0x141>(v)); v = fmaxf(v, dppf<0x140>(v));
  { const auto r = __builtin_amdgcn_permlane16_swap(__float_as_uint(v), __float_as_uint(v), false, false);
    v = fmaxf(__uint_as_float(r[0]), __uint_as_float(r[1])); }
  { const auto r = __builtin_amdgcn_permlane32_swap(__float_as_uint(v), __float_as_uint(v), false, false);
    v = fmaxf(__uint_as_float(r[0]), __uint_as_float(r[1])); }
  return v;
}

__global__ __launch_bounds__(512) void k1_prep(
    const uint16_t* __restrict__ x, const uint16_t* __restrict__ Are, const uint16_t* __restrict__ Aim,
    const uint16_t* __restrict__ Bre, const uint16_t* __restrict__ Bim, const uint16_t* __restrict__ Cre,
    const uint16_t* __restrict__ Cim, const uint16_t* __restrict__ logdt, const uint16_t* __restrict__ Dskip,
    char* __restrict__ ws) {
  extern __shared__ __attribute__((aligned(16))) char smem[];
  const int tid = threadIdx.x, lane = tid & 63;
  const int wv = __builtin_amdgcn_readfirstlane(tid >> 6);
  const int bid = blockIdx.x;
  STAMP_DECL
  const int ch = wv >> 2, t8 = tid & 255;
  const int l0 = ((bid & 7) * 32 + (bid >> 3)) * 8;
  {
    const uint16_t* pp = wv == 0 ? Are : wv == 1 ? Aim : wv == 2 ? Bre : wv == 3 ? Bim : wv == 4 ? Cre : wv == 5 ? Cim
                       : wv == 6 ? logdt : Dskip;
    glds16(pp + (wv < 6 ? bid * 2 * N_ + (lane & 31) * 8 : lane * 8), (unsigned)(size_t)(lds_char*)smem + K1_DUMP);
  }
  const u32x4 xr = *(const u32x4*)(x + (size_t)(l0 + wv) * D_ + lane * 8);
  STAMP(1);
  {
    char* ln = smem;
    *(u32x4*)(ln + (8 + wv) * K1_TS + lane * 16) = xr;
    f32x2 v[4];
#pragma unroll
    for (int j = 0; j < 4; ++j) v[j] = f32x2{bflo(xr[j]), bfhi(xr[j])};
    const f32x2 s2 = (v[0] + v[1]) + (v[2] + v[3]);
    const float mean = wave_sum(s2.x + s2.y) * (1.0f / D_);
    f32x2 q2 = f32x2{0.f, 0.f};
#pragma unroll
    for (int j = 0; j < 4; ++j) { const f32x2 dd = v[j] - mean; q2 = __builtin_elementwise_fma(dd, dd, q2); }
    const float var = wave_sum(q2.x + q2.y) * (1.0f / D_);
    const float m = rbf(mean);
    const float sd = rbf(__builtin_amdgcn_sqrtf(rbf(rbf(var) + 1e-5f)));
    const float r = 1.0f / sd;
    u32x4 uo;
#pragma unroll
    for (int j = 0; j < 4; ++j) {
      const uint32_t a = cvt2(v[j] - m);
      const uint32_t u = cvt2(f32x2{bflo(a), bfhi(a)} * r);
      uo[j] = pack2h(bflo(u), bfhi(u));
    }
    *(u32x4*)(ln + wv * K1_TS + lane * 16) = uo;
  }
  STAMP(3);
  __syncthreads();
  STAMP(4);
  {
    const char* ln = smem + ch * 8 * K1_TS + t8 * 4;
    uint32_t w[8];
#pragma unroll
    for (int r = 0; r < 8; ++r) w[r] = *(const uint32_t*)(ln + (ch == 0 ? 7 - r : r) * K1_TS);
    u32x4 a0, b0;
#pragma unroll
    for (int j = 0; j < 4; ++j) {
      a0[j] = __builtin_amdgcn_perm(w[2 * j + 1], w[2 * j], 0x05040100u);
      b0[j] = __builtin_amdgcn_perm(w[2 * j + 1], w[2 * j], 0x07060302u);
    }
    uint16_t* p0 = (uint16_t*)(ws + (ch == 0 ? WS_UR : WS_XT)) + (size_t)(2 * t8) * L_ + (ch == 0 ? (L_ - 8 - l0) : l0);
    *(u32x4*)(p0) = a0;
    *(u32x4*)(p0 + L_) = b0;
  }
  STAMP(5);
  STAMP_FLUSH(0);
}

constexpr int UC_S = 4160;
constexpr int UC_B4 = 4 * UC_S + 32;
constexpr int UREG = UC_B4 + 4 * UC_S;
constexpr int KOFF = UREG;
constexpr int T_OFF = KOFF + 512 + 4096;
constexpr int T_RS = 528;
constexpr int KF_OFF = T_OFF + 57 * T_RS;
constexpr int CH_LDS = KF_OFF + 3104;
constexpr int K2_LDS = 2 * CH_LDS + 1024;

__global__ __launch_bounds__(512) void k2_conv(
    const uint16_t* __restrict__ Are, const uint16_t* __restrict__ Aim, const uint16_t* __restrict__ Bre,
    const uint16_t* __restrict__ Bim, const uint16_t* __restrict__ Cre, const uint16_t* __restrict__ Cim,
    const uint16_t* __restrict__ logdt, const uint16_t* __restrict__ Dskip, const uint16_t* __restrict__ w1,
    const uint16_t* __restrict__ w2, char* __restrict__ ws) {
  extern __shared__ __attribute__((aligned(16))) char smem[];
  const int tid = threadIdx.x, lane = tid & 63;
  const int wv = __builtin_amdgcn_readfirstlane(tid >> 6);
  const int bid = blockIdx.x;
  STAMP_DECL
  const int m = lane & 15, kg = lane >> 4;
  const int chs = (wv >> 1) & 1, half = wv & 1;
  u32x2 xe[4], ue[4];
  uint16_t pds;
  int sexp;
  {
    const int ch = wv >> 2, wq = wv & 3, part = wq >> 1;
    const int t8 = tid & 255, n = tid & 127;
    const int d = 2 * bid + ch;
    char* cb = smem + ch * CH_LDS;
    char* tb = cb + T_OFF;
    float* Kf = (float*)(cb + KF_OFF);
    const uint16_t pAr = Are[d * N_ + n], pAi = Aim[d * N_ + n], pBr = Bre[d * N_ + n], pBi = Bim[d * N_ + n],
                   pCr = Cre[d * N_ + n], pCi = Cim[d * N_ + n], pdt = logdt[d];
    const uint16_t* uR = (const uint16_t*)(ws + WS_UR) + (size_t)d * L_;
    const u32x4 uu0 = *(const u32x4*)(uR + t8 * 8);
    u32x4 uu1 = *(const u32x4*)(uR + (t8 < 255 ? t8 + 1 : 255) * 8);
    {
      const int dc = 2 * bid + chs;
      const uint16_t* uC = (const uint16_t*)(ws + WS_UR) + (size_t)dc * L_;
      const uint16_t* xC = (const uint16_t*)(ws + WS_XT) + (size_t)dc * L_;
#pragma unroll
      for (int j = 0; j < 4; ++j) {
        const int t0 = 256 * (4 * half + j) + 16 * m + 4 * kg;
        xe[j] = *(const u32x2*)(xC + t0);
        ue[j] = *(const u32x2*)(uC + (2044 - t0));
      }
      pds = Dskip[dc];
    }
    if (t8 < 32) *(u32x4*)(cb + KOFF + t8 * 16) = u32x4{0, 0, 0, 0};
    else if (t8 < 40) {
      const int j = t8 - 32;
      *(u32x4*)(cb + (j & 3) * UC_S + (j >> 2) * UC_B4 + 256 * 16) = u32x4{0, 0, 0, 0};
    }
    const float dt = rbf(expf(bf2f(pdt)));
    const float zx = dt * bf2f(pAr);
    const float zy = dt * bf2f(pAi);
    f32x2 c;
    {
      const float br = dt * bf2f(pBr), bi = dt * bf2f(pBi);
      const float cr = bf2f(pCr), ci = bf2f(pCi);
      c = f32x2{cr * br - ci * bi, -(cr * bi + ci * br)};
    }
    {
      const float mg = wave_max(fmaxf(fabsf(c.x), fabsf(c.y)) * fmaxf(1.f, __expf(2048.f * zx)));
      if (lane == 0 && part == 0) Kf[772 + (wq & 1)] = mg;
    }
    auto putn = [&](auto NV, const int* rows, const f32x2* v) {
      constexpr int nv = decltype(NV)::value;
      uint32_t H[nv];
#pragma unroll
      for (int i = 0; i < nv; ++i) H[i] = cvt2h(v[i]);
#pragma unroll
      for (int i = 0; i < nv; ++i) *(uint32_t*)(tb + rows[i] * T_RS + 4 * n) = H[i];
    };
    auto cmuln = [&](auto NV, f32x2* o, const f32x2* v, f32x2 mm) {
      constexpr int nv = decltype(NV)::value;
      const f32x2 mn = f32x2{-mm.y, mm.x};
      f32x2 t[nv];
#pragma unroll
      for (int i = 0; i < nv; ++i) t[i] = f32x2{v[i].x, v[i].x} * mm;
#pragma unroll
      for (int i = 0; i < nv; ++i) o[i] = __builtin_elementwise_fma(f32x2{v[i].y, v[i].y}, mn, t[i]);
    };
    using I4 = std::integral_constant<int, 4>;
    using I8 = std::integral_constant<int, 8>;
    using I6 = std::integral_constant<int, 6>;
    using I3 = std::integral_constant<int, 3>;
    STAMP(1);
    const f32x2 g1 = cexp_(128.f * zx, -128.f * zy);
    const f32x2 ea = cexp_(part ? 2.f * zx : zx, part ? -2.f * zy : -zy);
    __syncthreads();
    {
      const f32x2 m2 = *(const f32x2*)(Kf + 772);
      const int ex = (int)((__float_as_uint(fmaxf(m2.x, m2.y)) >> 23) & 255u) - 127;
      sexp = 13 - ex;
      sexp = sexp > 126 ? 126 : (sexp < -126 ? -126 : sexp);
      c *= __uint_as_float((uint32_t)(127 + sexp) << 23);
    }
    STAMP(2);
    if (part == 0) {
      const f32x2 e1 = ea;
      const f32x2 e8 = cexp_(8.f * zx, 8.f * zy);
      const f32x2 e2 = cmul(e1, e1), e16 = cmul(e8, e8);
      const f32x2 e4 = cmul(e2, e2), e32 = cmul(e16, e16);
      f32x2 A[8], Bv[4];
      A[0] = c; A[1] = cmul(c, e1); A[2] = cmul(c, e2);
      Bv[0] = f32x2{1.f, 0.f}; Bv[1] = e8; Bv[2] = e16; Bv[3] = cmul(e16, e8);
      A[3] = cmul(A[1], e2);
      cmuln(I4{}, A + 4, A, e4);
      { const int r[4] = {41, 42, 43, 44}; putn(I4{}, r, Bv); }
      cmuln(I4{}, Bv, Bv, e32);
      { const int r[8] = {0, 1, 2, 3, 4, 5, 6, 7}; putn(I8{}, r, A); }
      cmuln(I8{}, A, A, g1);
      { const int r[4] = {45, 46, 47, 48}; putn(I4{}, r, Bv); }
      cmuln(I4{}, Bv, Bv, e32);
      { const int r[8] = {8, 9, 10, 11, 12, 13, 14, 15}; putn(I8{}, r, A); }
      { const int r[4] = {49, 50, 51, 52}; putn(I4{}, r, Bv); }
      cmuln(I4{}, Bv, Bv, e32);
      { const int r[4] = {53, 54, 55, 56}; putn(I4{}, r, Bv); }
    } else {
      const f32x2 e2 = ea;
      const f32x2 g2 = cmul(g1, g1), e4 = cmul(e2, e2);
      const f32x2 g4 = cmul(g2, g2);
      f32x2 Q[8], R[6], S[4];
      Q[0] = cmul(c, g2);
      Q[1] = cmul(Q[0], g1); Q[2] = cmul(Q[0], g2);
      Q[3] = cmul(Q[2], g1);
      cmuln(I4{}, Q + 4, Q, g4);
      cmuln(I6{}, R, Q, e4);
      { f32x2 t[4] = {Q[0], R[0], Q[1], R[1]}; cmuln(I4{}, S, t, e2); }
      { const int r[8] = {16, 20, 24, 26, 28, 30, 32, 33}; putn(I8{}, r, Q); }
      cmuln(I4{}, Q, Q + 4, g4);
      { const int r[6] = {18, 22, 25, 27, 29, 31}; putn(I6{}, r, R); }
      cmuln(I3{}, Q + 4, Q, g4);
      { const int r[4] = {17, 19, 21, 23}; putn(I4{}, r, S); }
      { const int r[4] = {34, 35, 36, 37}; putn(I4{}, r, Q); }
      { const int r[3] = {38, 39, 40}; putn(I3{}, r, Q + 4); }
    }
    __builtin_amdgcn_sched_barrier(0);
    STAMP(12);
    {
      if (t8 == 255) uu1 = u32x4{0, 0, 0, 0};
      const uint32_t S[8] = {uu0[0], uu0[1], uu0[2], uu0[3], uu1[0], uu1[1], uu1[2], uu1[3]};
#pragma unroll
      for (int ml = 0; ml < 8; ++ml) {
        const int s = 7 - ml;
        u32x4 o;
#pragma unroll
        for (int i = 0; i < 4; ++i)
          o[i] = (s & 1) ? __builtin_amdgcn_alignbit(S[(s >> 1) + i + 1], S[(s >> 1) + i], 16) : S[(s >> 1) + i];
        *(u32x4*)(cb + (ml & 3) * UC_S + (ml >> 2) * UC_B4 + t8 * 16) = o;
      }
    }
    STAMP(3);
    __syncthreads();
    STAMP(4);
    if (wq < 3) {
      const char* ta = tb + (16 * wq + m) * T_RS + kg * 16;
      const char* tq = tb + (41 + m) * T_RS + kg * 16;
      f32x4 a1 = f32x4{0, 0, 0, 0}, a2 = f32x4{0, 0, 0, 0};
      f16x8 Ah[8], Bh[8];
#pragma unroll
      for (int s = 0; s < 8; ++s) { Ah[s] = *(const f16x8*)(ta + 64 * s); Bh[s] = *(const f16x8*)(tq + 64 * s); }
      __builtin_amdgcn_sched_barrier(0);
#pragma unroll
      for (int s = 0; s < 8; s += 2) {
        a1 = __builtin_amdgcn_mfma_f32_16x16x32_f16(Ah[s], Bh[s], a1, 0, 0, 0);
        a2 = __builtin_amdgcn_mfma_f32_16x16x32_f16(Ah[s + 1], Bh[s + 1], a2, 0, 0, 0);
      }
      a1 += a2;
      float mx = 0.f;
#pragma unroll
      for (int j = 0; j < 4; ++j) {
        const int ix = (16 * wq + 4 * kg + j) * 16 + m;
        Kf[ix] = a1[j];
        if (ix <= 640) mx = fmaxf(mx, fabsf(a1[j]));
      }
      mx = wave_max(mx);
      if (lane == 0) Kf[768 + wq] = mx;
    }
    if (wv >= 4) {
      const char* src = (const char*)w1 + (size_t)(bid >> 3) * 16 * (F_ * 2) + (bid & 1) * F_;
      const unsigned dump = (unsigned)(size_t)(lds_char*)smem + 2 * CH_LDS;
#pragma unroll
      for (int j = 0; j < 7; ++j) {
        const unsigned c = (wq * 7 + j) * 64 + lane, r = c / 112u;
        glds16(src + r * (F_ * 2) + (c - r * 112u) * 16u, dump);
      }
#pragma unroll
      for (int j = 0; j < 2; ++j)
        glds16((const char*)w2 + (size_t)bid * 7168 + min(wq * 2 + j, 6) * 1024 + lane * 16, dump);
    }
    STAMP(5);
    __syncthreads();
    STAMP(6);
    {
      const int a = t8 >> 4, b = t8 & 15;
      const float* kp = Kf + k_rowbase(a) * 16 + b;
      float kv[8];
#pragma unroll
      for (int i = 0; i < 8; ++i) kv[i] = kp[16 * i];
      float vn = Kf[k_rowbase((t8 + 1) >> 4) * 16 + ((t8 + 1) & 15)];
      const f32x4 m4 = *(const f32x4*)(Kf + 768);
      __builtin_amdgcn_sched_barrier(0);
      const float mx = fmaxf(fmaxf(m4[0], m4[1]), m4[2]);
      const int ex = (int)((__float_as_uint(mx) >> 23) & 255u) - 127;
      int sh = 14 - ex;
      sh = sh > 126 ? 126 : (sh < -126 ? -126 : sh);
      const float scale = __uint_as_float((uint32_t)(127 + sh) << 23);
      if (t8 == 0) {
        int e = -(sh + sexp);
        e = e > 126 ? 126 : (e < -126 ? -126 : e);
        Kf[774] = __uint_as_float((uint32_t)(127 + e) << 23);
      }
      float v[8];
      {
        const bool c2 = a < 2, c4 = a < 4, c8 = a < 8;
        const float w0 = kv[0], w1_ = kv[1], w2_ = kv[2], w3 = kv[3];
        v[0] = w0;
        v[1] = c2 ? w1_ : w0;
        v[2] = c2 ? w2_ : c4 ? w1_ : w0;
        v[3] = c2 ? w3 : c4 ? w2_ : c8 ? w1_ : w0;
        v[4] = c2 ? kv[4] : c4 ? w2_ : c8 ? w1_ : ((t8 & 1) ? vn : w0);
        v[5] = c2 ? kv[5] : c4 ? w2_ : c8 ? w1_ : vn;
        v[6] = c2 ? kv[6] : c4 ? w3 : vn;
        v[7] = c2 ? kv[7] : vn;
#pragma unroll
        for (int i = 0; i < 8; ++i) v[i] *= scale;
      }
      const u32x4 kk = u32x4{pack2h(v[0], v[1]), pack2h(v[2], v[3]), pack2h(v[4], v[5]), pack2h(v[6], v[7])};
      *(u32x4*)(cb + KOFF + 512 + t8 * 16) = kk;
    }
    STAMP(7);
    __syncthreads();
    STAMP(8);
  }
  if (wv >= 4) { WAIT_VM(0); return; }

  char* cb = smem + chs * CH_LDS;
  const int ml = m & 7, mh = m >> 3;
  const char* abase = cb + (ml & 3) * UC_S + (ml >> 2) * UC_B4 + 16 * (253 - mh + kg) - 256 * half;
  const char* bbase = cb + KOFF + 512 + 32 * m + 16 * kg - 32 - 256 * half;
  f32x4 acc[8];
#pragma unroll
  for (int t = 0; t < 8; ++t) acc[t] = f32x4{0, 0, 0, 0};
  f16x8 af[2][8], bfr[2][8];
  af[0][0] = *(const f16x8*)(abase);
#pragma unroll
  for (int j = 0; j < 8; ++j) bfr[0][j] = *(const f16x8*)(bbase + 512 * j);
#pragma unroll
  for (int j = 1; j < 8; ++j) af[0][j] = *(const f16x8*)(abase - 512 * j);
  const float kinv = ((const float*)(cb + KF_OFF))[774];
  uint32_t ud[4][2];
  {
    const float dsk = bf2f(pds);
#pragma unroll
    for (int j = 0; j < 4; ++j)
#pragma unroll
      for (int r2 = 0; r2 < 2; ++r2) {
        const uint32_t uw = ue[j][1 - r2];
        const f16x2 u2 = __builtin_bit_cast(f16x2, uw);
        ud[j][r2] = cvt2(f32x2{(float)u2[1], (float)u2[0]} * dsk);
        asm volatile("" : "+v"(ud[j][r2]));
      }
  }
  __builtin_amdgcn_sched_barrier(0);
#pragma unroll
  for (int s = 0; s < 4; ++s) {
    int slot = 0;
#pragma unroll
    for (int al = 0; al < 8; ++al)
#pragma unroll
      for (int T = al; T < 8; ++T) {
        acc[T] = __builtin_amdgcn_mfma_f32_16x16x32_f16(af[s & 1][al], bfr[s & 1][T - al], acc[T], 0, 0, 0);
        __builtin_amdgcn_sched_barrier(0);
        if (s < 3 && (slot & 1) == 0 && slot < 32) {
          const int j = slot >> 1;
          if (j >= 1 && j <= 8) bfr[(s + 1) & 1][j - 1] = *(const f16x8*)(bbase + 512 * (j - 1) - 64 * (s + 1));
          else { const int ja = j == 0 ? 0 : j - 8; af[(s + 1) & 1][ja] = *(const f16x8*)(abase - 512 * ja - 64 * (s + 1)); }
          __builtin_amdgcn_sched_barrier(0);
        }
        ++slot;
      }
  }
  STAMP(9);
  char* xb = cb + T_OFF;
  f32x4 keep[4];
  if (half) {
#pragma unroll
    for (int j = 0; j < 4; ++j) { *(f32x4*)(xb + 4096 + j * 1024 + lane * 16) = acc[j]; keep[j] = acc[4 + j]; }
    asm volatile("" ::: "memory");
  } else {
#pragma unroll
    for (int j = 0; j < 4; ++j) { *(f32x4*)(xb + j * 1024 + lane * 16) = acc[4 + j]; keep[j] = acc[j]; }
    asm volatile("" ::: "memory");
  }
  __syncthreads();
  STAMP(10);
  uint16_t* hT = (uint16_t*)(ws + WS_HT) + (size_t)(2 * bid + chs) * L_;
#pragma unroll
  for (int j = 0; j < 4; ++j) {
    const f32x4 o = *(const f32x4*)(xb + (1 - half) * 4096 + j * 1024 + lane * 16);
    const f32x4 a = keep[j];
    u32x2 hv;
#pragma unroll
    for (int r2 = 0; r2 < 2; ++r2) {
      const uint32_t y = cvt2((f32x2{a[2 * r2], a[2 * r2 + 1]} + f32x2{o[2 * r2], o[2 * r2 + 1]}) * kinv);
      const uint32_t t = cvt2(f32x2{bflo(y), bfhi(y)} + f32x2{bflo(ud[j][r2]), bfhi(ud[j][r2])});
      hv[r2] = cvt2(f32x2{bflo(t), bfhi(t)} + f32x2{bflo(xe[j][r2]), bfhi(xe[j][r2])});
    }
    *(u32x2*)(hT + 256 * (4 * half + j) + 16 * m + 4 * kg) = hv;
  }
  STAMP(11);
  STAMP_FLUSH(1);
}

constexpr int G1_HS = 64 * 256;
constexpr int G1_WS = 64 * 224;
constexpr int G1_STAGE = G1_HS + G1_WS;
constexpr int G1_NS = 5;
constexpr int G1_ST = G1_NS * G1_STAGE;
constexpr int G1_DUMP = G1_ST + 2048;
constexpr int K3_LDS = G1_DUMP + 1024;
#define G1_LA 2
#define PF2_A 2
__device__ constexpr int g1_pfs(int kt) {
  return kt <= -1 ? 0 : kt <= 5 ? PF2_A * (kt + 1) : kt == 6 ? PF2_A * 6 + (28 - PF2_A * 6) / 2 : 28;
}

__device__ __forceinline__ bf16x4 tr_read(const char* p) {
  return __builtin_amdgcn_ds_read_tr16_b64_v4i16((lds_bf16x4*)p);
}
__device__ __forceinline__ bf16x8 cat8(bf16x4 a, bf16x4 b) {
  return bf16x8{a[0], a[1], a[2], a[3], b[0], b[1], b[2], b[3]};
}
__device__ __forceinline__ void swap16(uint32_t& a, uint32_t& b) {
  auto r = __builtin_amdgcn_permlane16_swap(a, b, false, false);
  a = r[0]; b = r[1];
}
__device__ __forceinline__ float sum_g(float t) {
  { const auto r = __builtin_amdgcn_permlane16_swap(__float_as_uint(t), __float_as_uint(t), false, false);
    t = __uint_as_float(r[0]) + __uint_as_float(r[1]); }
  { const auto r = __builtin_amdgcn_permlane32_swap(__float_as_uint(t), __float_as_uint(t), false, false);
    t = __uint_as_float(r[0]) + __uint_as_float(r[1]); }
  return t;
}
#define WAIT_VM_N(n) asm volatile("s_waitcnt vmcnt(%0)" :: "i"(n) : "memory")
__device__ __forceinline__ void glds16s(unsigned voff, const void* sbase, unsigned lds_dst) {
  asm volatile("s_mov_b32 m0, %2\n\ts_nop 0\n\tglobal_load_lds_dwordx4 %0, %1" :: "v"(voff), "s"(sbase), "s"(lds_dst)
               : "memory");
}

template <int WF>
__device__ __forceinline__ void k3_body(char* __restrict__ ws, char* smem,
                                        const int lane, const int wv, const int l0, const int f0, const int ft) {
  constexpr int NT = WF ? 3 : 4;
  constexpr int CI0 = WF ? 4 : 0;
  STAMP_DECL
  const int wl = wv & 3;
  const unsigned lbase = (unsigned)(size_t)(lds_char*)smem;
  const int g = lane >> 4, q = (lane >> 2) & 3, pp = lane & 3, r15 = lane & 15;
  int tix[NT];
#pragma unroll
  for (int i = 0; i < NT; ++i) {
    int t = i + (WF ? (wl == 3 ? 0 : wl) : wl);
    t = t >= NT ? t - NT : t;
    tix[i] = CI0 + t;
  }
  const int xv = 2 * (4 * (g & 1) + q);
  unsigned hb[3][2], wb[3][NT];
#pragma unroll
  for (int r = 0; r < 3; ++r) {
#pragma unroll
    for (int cc = 0; cc < 2; ++cc) {
      hb[r][cc] = lbase + r * 2 * G1_STAGE + (4 * g + q) * 256 + ((((2 * (2 * wl + cc)) ^ xv) + (pp >> 1)) * 16) + 8 * (pp & 1);
      asm volatile("" : "+v"(hb[r][cc]));
    }
#pragma unroll
    for (int i = 0; i < NT; ++i) {
      wb[r][i] = lbase + r * 2 * G1_STAGE + G1_HS + (4 * g + q) * 224 + 8 * pp + 32 * tix[i];
      asm volatile("" : "+v"(wb[r][i]));
    }
  }
  const int lrow = l0 + 32 * wl + r15;

  f32x4 acc[NT][2];
#pragma unroll
  for (int i = 0; i < NT; ++i) { acc[i][0] = f32x4{0, 0, 0, 0}; acc[i][1] = f32x4{0, 0, 0, 0}; }
  f32x4 accS[2], accG[2], accC;
  accS[0] = accS[1] = accG[0] = accG[1] = accC = f32x4{0, 0, 0, 0};
  bf16x8 ones = {0x3f80, 0x3f80, 0x3f80, 0x3f80, 0x3f80, 0x3f80, 0x3f80, 0x3f80};
  asm volatile("" : "+v"(ones));

  struct Frag { bf16x8 h[2], w[NT]; };
  auto trl = [&](unsigned a) { return __builtin_amdgcn_ds_read_tr16_b64_v4i16((lds_bf16x4*)(size_t)a); };
  auto ld1 = [&](Frag& F, int kt, int ks, int i) {
    const int bf = kt % G1_NS, r = bf >> 1, o = (bf & 1) * G1_STAGE;
    if (i < 2) F.h[i] = cat8(trl(hb[r][i] + o + ks * 8192), trl(hb[r][i] + o + ks * 8192 + 4096));
    else F.w[i - 2] = cat8(trl(wb[r][i - 2] + o + ks * 32 * 224), trl(wb[r][i - 2] + o + (ks * 32 + 16) * 224));
  };
  constexpr int NM = 2 * NT + 1 + (WF ? 4 : 0);
  auto mm1 = [&](const Frag& F, int i) {
    if (i < 2 * NT) {
      const int ci = i >> 1, cc = i & 1;
      acc[ci][cc] = __builtin_amdgcn_mfma_f32_16x16x32_bf16(F.w[ci], F.h[cc], acc[ci][cc], 0, 0, 0);
    } else if (i == 2 * NT) {
      accC = __builtin_amdgcn_mfma_f32_16x16x32_bf16(F.w[0], ones, accC, 0, 0, 0);
    } else {
      const int cc = i & 1;
      if (i < 2 * NT + 3) accS[cc] = __builtin_amdgcn_mfma_f32_16x16x32_bf16(ones, F.h[cc], accS[cc], 0, 0, 0);
      else accG[cc] = __builtin_amdgcn_mfma_f32_16x16x32_bf16(F.h[cc], F.h[cc], accG[cc], 0, 0, 0);
    }
  };
  auto step = [&](const Frag& C, Frag& N, int nkt, int nks, int kstep) {
#pragma unroll
    for (int i = 0; i < NM; ++i) {
      mm1(C, kstep == 15 ? NM - 1 - i : i);
      __builtin_amdgcn_sched_barrier(0);
      if (nkt >= 0 && i < NT + 2) ld1(N, nkt, nks, i);
      __builtin_amdgcn_sched_barrier(0);
    }
    if (!WF && kstep < 2) {
      if (kstep == (ft & 1)) {
        asm volatile("" ::: "memory");
#pragma unroll
        for (int cc = 0; cc < 2; ++cc) {
          const u32x4 hv = __builtin_bit_cast(u32x4, C.h[cc]);
          uint32_t a0 = hv[0], a1 = hv[1], b0 = hv[2], b1 = hv[3];
          swap16(a0, b0); swap16(a1, b1);
          uint16_t* p = (uint16_t*)(ws + WS_HR) + (size_t)(lrow + 16 * cc) * D_ + 32 * ft + 16 * (g & 1) + 8 * (g >> 1);
          *(u32x4*)p = u32x4{a0, a1, b0, b1};
        }
      }
      __builtin_amdgcn_sched_barrier(0);
    }
  };
  Frag F0, F1;
  STAMP(1);
  RAW_BARRIER();
  STAMP(2);
#pragma unroll
  for (int i = 0; i < NT + 2; ++i) ld1(F0, 0, 0, i);
#pragma unroll
  for (int kt = 0; kt < 8; ++kt) {
    STAMP(3 + kt);
    step(F0, F1, kt, 1, 2 * kt);
    if (kt < 7) {
      if (G1_LA == G1_NS - 1 && kt + G1_NS < 8) WAIT_LGKM0();
      RAW_BARRIER();
    }
    step(F1, F0, kt < 7 ? kt + 1 : -1, 0, 2 * kt + 1);
  }
  STAMP(11);
  float* stt = (float*)(smem + G1_ST);
  float mm[2], rs[2];
  if (r15 == 0 && (!WF || wl < 3)) *(f32x4*)(stt + 256 + 16 * tix[0] + 4 * g) = accC;
  if (WF) {
    f32x2 sq, mean;
#pragma unroll
    for (int cc = 0; cc < 2; ++cc) {
      sq[cc] = (pp == 0) ? accG[cc][0] : (pp == 1) ? accG[cc][1] : (pp == 2) ? accG[cc][2] : accG[cc][3];
      mean[cc] = accS[cc][0];
    }
    mean *= (1.0f / D_);
    f32x2 var = sq * (1.0f / D_) - mean * mean;
    var = f32x2{fmaxf(var[0], 0.f), fmaxf(var[1], 0.f)};
    const uint32_t mb = cvt2(mean);
    uint32_t t = cvt2(var);
    t = cvt2(f32x2{bflo(t), bfhi(t)} + 1e-5f);
    t = cvt2(f32x2{__builtin_amdgcn_sqrtf(bflo(t)), __builtin_amdgcn_sqrtf(bfhi(t))});
    const float r0 = __builtin_amdgcn_rcpf(bflo(t)), r1 = __builtin_amdgcn_rcpf(bfhi(t));
    if (g == q) {
      *(f32x2*)(stt + 2 * (32 * wl + r15)) = f32x2{bflo(mb), r0};
      *(f32x2*)(stt + 2 * (32 * wl + 16 + r15)) = f32x2{bfhi(mb), r1};
    }
  }
  WAIT_LGKM0();
  RAW_BARRIER();
  __builtin_amdgcn_sched_barrier(0);
  f32x4 c4[NT];
#pragma unroll
  for (int ci = 0; ci < NT; ++ci) c4[ci] = *(const f32x4*)(stt + 256 + 16 * tix[ci] + 4 * g);
#pragma unroll
  for (int cc = 0; cc < 2; ++cc) {
    const f32x2 t = *(const f32x2*)(stt + 2 * (32 * wl + 16 * cc + r15));
    mm[cc] = t[0]; rs[cc] = t[1];
  }
  STAMP(12);
  uint16_t* H1 = (uint16_t*)(ws + WS_H1);
#pragma unroll
  for (int ci = 0; ci < NT; ++ci) {
    uint32_t pk[2][2];
#pragma unroll
    for (int cc = 0; cc < 2; ++cc)
#pragma unroll
      for (int j2 = 0; j2 < 2; ++j2) {
        const f32x2 a = {acc[ci][cc][2 * j2], acc[ci][cc][2 * j2 + 1]};
        const f32x2 c = {c4[ci][2 * j2], c4[ci][2 * j2 + 1]};
        const uint32_t vb = cvt2((a - mm[cc] * c) * rs[cc]);
        const f32x2 v = {bflo(vb), bfhi(vb)};
        const f32x2 e = v * -1.4426950408889634f;
        const f32x2 dn = f32x2{__builtin_amdgcn_exp2f(e[0]), __builtin_amdgcn_exp2f(e[1])} + 1.0f;
        const f32x2 o = v * f32x2{__builtin_amdgcn_rcpf(dn[0]), __builtin_amdgcn_rcpf(dn[1])};
        pk[cc][j2] = cvt2(o);
      }
    swap16(pk[0][0], pk[1][0]);
    swap16(pk[0][1], pk[1][1]);
    uint16_t* p = H1 + (size_t)(lrow + 16 * (g & 1)) * F_ + f0 + 16 * tix[ci] + 8 * (g >> 1);
    st16<SM3>(p, u32x4{pk[0][0], pk[0][1], pk[1][0], pk[1][1]});
    __builtin_amdgcn_sched_barrier(0);
  }
  STAMP(13);
  STAMP_FLUSH(2);
}

__global__ __launch_bounds__(640) void k3_gemm1(const uint16_t* __restrict__ w1, const uint16_t* __restrict__ w2,
                                                char* __restrict__ ws) {
  extern __shared__ __attribute__((aligned(16))) char smem[];
  const int tid = threadIdx.x, lane = tid & 63;
  const int wv = __builtin_amdgcn_readfirstlane(tid >> 6);
  const int bid = blockIdx.x;
  const int xcd = bid & 7, idx = bid >> 3;
  const int lt = 4 * (xcd >> 1) + (idx & 3), ft = 8 * (xcd & 1) + (idx >> 2);
  const int l0 = lt * 128, f0 = ft * 112;
  if (wv >= 8) {
    const unsigned lbase = (unsigned)(size_t)(lds_char*)smem;
    const uint16_t* hT = (const uint16_t*)(ws + WS_HT);
    const int kt0 = ft >> 1;
    const int lw = wv - 8;
    unsigned hoff[8], woff[7];
#pragma unroll
    for (int i = 0; i < 8; ++i) {
      const int row = (8 * lw + i) * 4 + (lane >> 4);
      hoff[i] = 2u * (row * L_ + l0 + 8 * ((lane & 15) ^ (2 * (row & 7))));
    }
#pragma unroll
    for (int i = 0; i < 7; ++i) {
      const int nn = (7 * lw + i) * 64 + lane;
      const int r2 = nn / 14, ch = nn - r2 * 14;
      woff[i] = 2u * (r2 * F_ + f0 + 8 * ch);
    }
    auto stage = [&](int kt) {
      const unsigned sb = lbase + (kt % G1_NS) * G1_STAGE;
      const int ka = (kt0 + kt) & 7;
      const uint16_t* hs = hT + (size_t)ka * 64 * L_;
      const uint16_t* wsrc = w1 + (size_t)ka * 64 * F_;
#pragma unroll
      for (int i = 0; i < 8; ++i) {
        glds16s(hoff[i], hs, sb + (8 * lw + i) * 1024);
        if (i < 7) glds16s(woff[i], wsrc, sb + G1_HS + (7 * lw + i) * 1024);
      }
    };
    const char* src = (const char*)w2 + (size_t)idx * 57344 + lw * 28672;
#pragma unroll
    for (int s = 0; s < G1_LA; ++s) stage(s);
#pragma unroll
    for (int kt = -1; kt < 7; ++kt) {
      WAIT_VM_N(((kt + G1_LA < 7 ? kt + G1_LA : 7) - kt - 1) * 15 + g1_pfs(kt) - g1_pfs(kt - G1_LA));
      RAW_BARRIER();
      if (kt + 1 + G1_LA < 8) stage(kt + 1 + G1_LA);
#pragma unroll
      for (int j = g1_pfs(kt); j < g1_pfs(kt + 1); ++j) glds16s(lane * 16u + j * 1024u, src, lbase + G1_DUMP);
    }
    RAW_BARRIER();
    WAIT_VM(0);
  } else if (wv < 4) k3_body<0>(ws, smem, lane, wv, l0, f0, ft);
  else k3_body<1>(ws, smem, lane, wv, l0, f0, ft);
}

constexpr int G2_HS = 64 * 256;
constexpr int G2_WS = 128 * 128;
constexpr int G2_STAGE = G2_HS + G2_WS;
constexpr int G2_NS = 5;
constexpr int G2_NK = F_ / 128;
constexpr int K4_LDS = G2_NS * G2_STAGE;

#define NLD 4
#define G2_LA 2
__global__ __launch_bounds__(256 + 64 * NLD) void k4_gemm2(const uint16_t* __restrict__ w2, char* __restrict__ ws,
                                                uint16_t* __restrict__ out) {
  extern __shared__ __attribute__((aligned(16))) char smem[];
  const int tid = threadIdx.x, lane = tid & 63;
  const int wv = __builtin_amdgcn_readfirstlane(tid >> 6);
  const int bid = blockIdx.x;
  const int xcd = bid & 7, idx = bid >> 3;
  STAMP_DECL
  const int lt = 4 * xcd + (idx & 3), dtile = idx >> 2;
  const int l0 = lt * 64, d0 = dtile * 64;
  const unsigned lbase = (unsigned)(size_t)(lds_char*)smem;
  const uint16_t* H1 = (const uint16_t*)(ws + WS_H1);
  if (wv >= 4) {
    constexpr int PP = 16 / NLD;
    const int p0 = (wv - 4) * PP;
    unsigned hoff[PP], woff[PP];
#pragma unroll
    for (int i = 0; i < PP; ++i) {
      const int row = (p0 + i) * 4 + (lane >> 4);
      hoff[i] = 2u * ((l0 + row) * F_ + 8 * ((lane & 15) ^ (row & 15)));
      const int r2 = (p0 + i) * 8 + (lane >> 3);
      const int gf = ((r2 >> 1) & 1) | (((r2 >> 3) & 1) << 1);
      woff[i] = 2u * (r2 * D_ + d0 + 8 * ((lane & 7) ^ (2 * gf)));
    }
    auto stage = [&](int kt) {
      const unsigned sb = lbase + (kt % G2_NS) * G2_STAGE + p0 * 1024;
#pragma unroll
      for (int i = 0; i < PP; ++i) {
        glds16s(woff[i], w2 + (size_t)kt * 128 * D_, sb + G2_HS + i * 1024);
        glds16s(hoff[i], H1 + kt * 128, sb + i * 1024);
      }
    };
#pragma unroll
    for (int s = 0; s < G2_LA; ++s) stage(s);
#pragma unroll
    for (int kt = -1; kt < G2_NK - 1; ++kt) {
      WAIT_VM_N(((kt + G2_LA < G2_NK - 1 ? kt + G2_LA : G2_NK - 1) - kt - 1) * 2 * PP);
      RAW_BARRIER();
      if (kt + 1 + G2_LA < G2_NK) stage(kt + 1 + G2_LA);
    }
    return;
  }
  const int g = lane >> 4, q = (lane >> 2) & 3, pp = lane & 3;
  const int wl = wv & 1, wd = wv >> 1;
  const int r15 = lane & 15;
  const size_t oo = (size_t)(l0 + 32 * wl + r15) * D_ + d0 + 32 * wd + 16 * (g & 1) + 8 * (g >> 1);
  u32x4 hres[2];
  hres[0] = *(const u32x4*)((const uint16_t*)(ws + WS_HR) + oo);
  hres[1] = *(const u32x4*)((const uint16_t*)(ws + WS_HR) + oo + 16 * D_);

  int hrd[4];
#pragma unroll
  for (int ks = 0; ks < 4; ++ks) hrd[ks] = (32 * wl + r15) * 256 + (((4 * ks + g) ^ r15) * 16);
  int wrd[2][2];
#pragma unroll
  for (int ci = 0; ci < 2; ++ci)
#pragma unroll
    for (int t = 0; t < 2; ++t) {
      const int row = 8 * g + 4 * t + q;
      const int gf = ((row >> 1) & 1) | (((row >> 3) & 1) << 1);
      wrd[ci][t] = G2_HS + row * 128 + (((2 * (2 * wd + ci)) ^ (2 * gf)) + (pp >> 1)) * 16 + 8 * (pp & 1);
    }
  f32x4 acc[2][2];
  acc[0][0] = acc[0][1] = acc[1][0] = acc[1][1] = f32x4{0, 0, 0, 0};

  struct Frag { bf16x8 h[2], w[2]; };
  auto ldfrag = [&](Frag& F, int kt, int ks) {
    const char* sb = smem + (kt % G2_NS) * G2_STAGE;
    F.h[0] = *(const bf16x8*)(sb + hrd[ks]);
    F.w[0] = cat8(tr_read(sb + wrd[0][0] + ks * 4096), tr_read(sb + wrd[0][1] + ks * 4096));
    F.w[1] = cat8(tr_read(sb + wrd[1][0] + ks * 4096), tr_read(sb + wrd[1][1] + ks * 4096));
    F.h[1] = *(const bf16x8*)(sb + hrd[ks] + 4096);
    __builtin_amdgcn_sched_barrier(0);
  };
  auto mma = [&](const Frag& F) {
#pragma unroll
    for (int a = 0; a < 2; ++a)
#pragma unroll
      for (int ci = 0; ci < 2; ++ci)
        acc[a][ci] = __builtin_amdgcn_mfma_f32_16x16x32_bf16(F.w[ci], F.h[a], acc[a][ci], 0, 0, 0);
    __builtin_amdgcn_sched_barrier(0);
  };
  Frag F0, F1, F2, F3;
  STAMP(1);
  RAW_BARRIER();
  STAMP(2);
  ldfrag(F0, 0, 0);
  ldfrag(F1, 0, 1);
#pragma unroll
  for (int kt = 0; kt < G2_NK; ++kt) {
    if ((kt & 1) == 0) STAMP(3 + kt / 2);
    ldfrag(F2, kt, 2);
    mma(F0);
    ldfrag(F3, kt, 3);
    mma(F1);
    if (kt < G2_NK - 1) {
      if (G2_LA == 4 && kt + 5 < G2_NK) WAIT_LGKM0();
      RAW_BARRIER();
      ldfrag(F0, kt + 1, 0);
    }
    mma(F2);
    if (kt < G2_NK - 1) ldfrag(F1, kt + 1, 1);
    mma(F3);
  }
  STAMP(10);
#pragma unroll
  for (int a = 0; a < 2; ++a) {
    uint32_t a0 = pack2(acc[a][0][0], acc[a][0][1]), a1 = pack2(acc[a][0][2], acc[a][0][3]);
    uint32_t b0 = pack2(acc[a][1][0], acc[a][1][1]), b1 = pack2(acc[a][1][2], acc[a][1][3]);
    swap16(a0, b0); swap16(a1, b1);
    const uint32_t v[4] = {a0, a1, b0, b1};
    u32x4 r;
#pragma unroll
    for (int j = 0; j < 4; ++j) r[j] = pack2(bflo(hres[a][j]) + bflo(v[j]), bfhi(hres[a][j]) + bfhi(v[j]));
    *(u32x4*)(out + oo + a * 16 * D_) = r;
  }
  STAMP(11);
  STAMP_FLUSH(3);
}

extern "C" void kernel_launch(void* const* d_in, const int* in_sizes, int n_in,
                              void* d_out, int out_size, void* d_ws, size_t ws_size,
                              hipStream_t stream) {
  const uint16_t* x = (const uint16_t*)d_in[0];
  const uint16_t* Are = (const uint16_t*)d_in[1];
  const uint16_t* Aim = (const uint16_t*)d_in[2];
  const uint16_t* Bre = (const uint16_t*)d_in[3];
  const uint16_t* Bim = (const uint16_t*)d_in[4];
  const uint16_t* Cre = (const uint16_t*)d_in[5];
  const uint16_t* Cim = (const uint16_t*)d_in[6];
  const uint16_t* logdt = (const uint16_t*)d_in[7];
  const uint16_t* Dskip = (const uint16_t*)d_in[8];
  const uint16_t* w1 = (const uint16_t*)d_in[10];
  const uint16_t* w2 = (const uint16_t*)d_in[11];
  char* ws = (char*)d_ws;
  (void)hipFuncSetAttribute((const void*)k1_prep, hipFuncAttributeMaxDynamicSharedMemorySize, K1_LDS);
  (void)hipFuncSetAttribute((const void*)k2_conv, hipFuncAttributeMaxDynamicSharedMemorySize, K2_LDS);
  (void)hipFuncSetAttribute((const void*)k3_gemm1, hipFuncAttributeMaxDynamicSharedMemorySize, K3_LDS);
  (void)hipFuncSetAttribute((const void*)k4_gemm2, hipFuncAttributeMaxDynamicSharedMemorySize, K4_LDS);
  k1_prep<<<256, 512, K1_LDS, stream>>>(x, Are, Aim, Bre, Bim, Cre, Cim, logdt, Dskip, ws);
  k2_conv<<<D_ / 2, 512, K2_LDS, stream>>>(Are, Aim, Bre, Bim, Cre, Cim, logdt, Dskip, w1, w2, ws);
  k3_gemm1<<<256, 640, K3_LDS, stream>>>(w1, w2, ws);
  k4_gemm2<<<256, 256 + 64 * NLD, K4_LDS, stream>>>(w2, ws, (uint16_t*)d_out);
}
```

```cpp
#include <hip/hip_runtime.h>
#include <hip/hip_bf16.h>
#include <stdint.h>
#include <type_traits>

constexpr int L_ = 2048, D_ = 512, N_ = 128, F_ = 1792;

typedef __attribute__((ext_vector_type(8))) short bf16x8;
typedef __attribute__((ext_vector_type(4))) short bf16x4;
typedef __attribute__((ext_vector_type(2))) short bf16x2_t;
typedef __attribute__((ext_vector_type(8))) _Float16 f16x8;
typedef __attribute__((ext_vector_type(2))) _Float16 f16x2;
typedef __attribute__((ext_vector_type(4))) float f32x4;
typedef __attribute__((ext_vector_type(2))) float f32x2;
typedef __attribute__((ext_vector_type(4))) unsigned u32x4;
typedef __attribute__((ext_vector_type(2))) unsigned u32x2;
typedef __attribute__((address_space(3))) bf16x4 lds_bf16x4;
typedef __attribute__((address_space(3))) char lds_char;

__device__ __forceinline__ float bf2f(uint16_t v) { return __uint_as_float(((uint32_t)v) << 16); }
__device__ __forceinline__ float bflo(uint32_t v) { return __uint_as_float(v << 16); }
__device__ __forceinline__ float bfhi(uint32_t v) { return __uint_as_float(v & 0xffff0000u); }
__device__ __forceinline__ uint16_t f2bf(float f) {
  __hip_bfloat16 b = __float2bfloat16(f);
  return *reinterpret_cast<uint16_t*>(&b);
}
__device__ __forceinline__ float rbf(float f) { return bf2f(f2bf(f)); }
__device__ __forceinline__ uint32_t pack2(float lo, float hi) { return (uint32_t)f2bf(lo) | ((uint32_t)f2bf(hi) << 16); }
__device__ __forceinline__ uint32_t pack2h(float lo, float hi) {
  f16x2 v; v[0] = (_Float16)lo; v[1] = (_Float16)hi;
  return __builtin_bit_cast(uint32_t, v);
}

__device__ __forceinline__ void glds16(const void* g, unsigned lds_dst) {
  unsigned keep;
  asm volatile("s_mov_b32 %0, m0\n\ts_mov_b32 m0, %2\n\ts_nop 0\n\tglobal_load_lds_dwordx4 %1, off\n\ts_mov_b32 m0, %0"
               : "=&s"(keep) : "v"(g), "s"(lds_dst) : "memory");
}
template <int M> __device__ __forceinline__ void st16(void* p, u32x4 v) {
  if (M == 0) *(u32x4*)p = v;
  else if (M == 1) asm volatile("global_store_dwordx4 %0, %1, off nt\n\ts_nop 1" :: "v"(p), "v"(v) : "memory");
  else if (M == 2) asm volatile("global_store_dwordx4 %0, %1, off sc1\n\ts_nop 1" :: "v"(p), "v"(v) : "memory");
  else if (M == 3) asm volatile("global_store_dwordx4 %0, %1, off sc0 sc1\n\ts_nop 1" :: "v"(p), "v"(v) : "memory");
  else asm volatile("global_store_dwordx4 %0, %1, off sc1 nt\n\ts_nop 1" :: "v"(p), "v"(v) : "memory");
}
#define SM1 0
#define SM1K 0
#define GSTAG 12
#define GBAR 2
#define GST0 0
#define SM4 0
#define SM3 0
#define WAIT_VM(n) asm volatile("s_waitcnt vmcnt(" #n ")" ::: "memory")
#define WAIT_LGKM0() asm volatile("s_waitcnt lgkmcnt(0)" ::: "memory")
#define RAW_BARRIER() do { asm volatile("" ::: "memory"); __builtin_amdgcn_s_barrier(); asm volatile("" ::: "memory"); } while (0)

#define STAMP_DECL
#define STAMP(i)
#define STAMP_FLUSH(k)
constexpr size_t WS_UR = 0;
constexpr size_t WS_XT = WS_UR + (size_t)D_ * L_ * 2;
constexpr size_t WS_HT = WS_XT + (size_t)D_ * L_ * 2;
constexpr size_t WS_KG = WS_HT + (size_t)D_ * L_ * 2;
constexpr size_t WS_HR = WS_KG + (size_t)D_ * L_ * 2;
constexpr size_t WS_KS = WS_HR + (size_t)D_ * L_ * 2;
constexpr size_t WS_CS = WS_KS + 4096;
constexpr size_t WS_H1 = WS_CS + 8192;
constexpr size_t WS_ST = WS_UR;

#define CSZ 11200, 14592, 9216
constexpr int CSA[3] = {CSZ};
constexpr int CS2 = CSA[0], CS3 = CSA[1], CS4 = CSA[2];
constexpr int K1_DUMP = 0;
constexpr int K1_LDS = K1_DUMP + 1024;

typedef __attribute__((ext_vector_type(2))) __bf16 bf16v2;
__device__ __forceinline__ uint32_t cvt2(f32x2 v) {
  const bf16v2 b = __builtin_convertvector(v, bf16v2);
  return __builtin_bit_cast(uint32_t, b);
}
__device__ __forceinline__ uint32_t cvt2h(f32x2 v) {
  const f16x2 b = __builtin_convertvector(v, f16x2);
  return __builtin_bit_cast(uint32_t, b);
}
__device__ __forceinline__ f32x2 cmul(f32x2 a, f32x2 b) {
  const f32x2 t = f32x2{a.x, a.x} * b;
  return __builtin_elementwise_fma(f32x2{a.y, a.y}, f32x2{-b.y, b.x}, t);
}
__device__ __forceinline__ f32x2 cexp_(float x, float y) {
  const float m = __expf(x);
  const float hi = y * 0.15915493667125702f;
  const float k = rintf(hi);
  float fr = fmaf(y, 0.15915493667125702f, -k);
  fr = fmaf(y, 6.4206382432985265e-09f, fr);
  return f32x2{m * __builtin_amdgcn_cosf(fr), m * __builtin_amdgcn_sinf(fr)};
}
__device__ __forceinline__ int k_rowbase(int a) { return a < 2 ? 8 * a : a < 4 ? 8 + 4 * a : a < 8 ? 16 + 2 * a : 24 + a; }
template <int C> __device__ __forceinline__ float dppf(float v) {
  return __builtin_bit_cast(float, __builtin_amdgcn_update_dpp(0, __builtin_bit_cast(int, v), C, 0xf, 0xf, true));
}
__device__ __forceinline__ float wave_sum(float v) {
  v += dppf<0xB1>(v); v += dppf<0x4E>(v); v += dppf<0x141>(v); v += dppf<0x140>(v);
  { const auto r = __builtin_amdgcn_permlane16_swap(__float_as_uint(v), __float_as_uint(v), false, false);
    v = __uint_as_float(r[0]) + __uint_as_float(r[1]); }
  { const auto r = __builtin_amdgcn_permlane32_swap(__float_as_uint(v), __float_as_uint(v), false, false);
    v = __uint_as_float(r[0]) + __uint_as_float(r[1]); }
  return v;
}
__device__ __forceinline__ float wave_max(float v) {
  v = fmaxf(v, dppf<0xB1>(v)); v = fmaxf(v, dppf<0x4E>(v)); v = fmaxf(v, dppf<0x141>(v)); v = fmaxf(v, dppf<0x140>(v));
  { const auto r = __builtin_amdgcn_permlane16_swap(__float_as_uint(v), __float_as_uint(v), false, false);
    v = fmaxf(__uint_as_float(r[0]), __uint_as_float(r[1])); }
  { const auto r = __builtin_amdgcn_permlane32_swap(__float_as_uint(v), __float_as_uint(v), false, false);
    v = fmaxf(__uint_as_float(r[0]), __uint_as_float(r[1])); }
  return v;
}

struct PtrRec { const void* p[16]; };
__device__ __attribute__((aligned(128))) PtrRec g_rec[8];
typedef const __attribute__((address_space(4))) uint64_t* crec_t;
#define REC_PTR(T, i) ((T)(__attribute__((address_space(1))) char*)((crec_t)(const void*)&g_rec[blockIdx.x & 7])[i])
extern "C" __global__ __launch_bounds__(576) void k1_prep(
    const uint16_t* __restrict__ x, const uint16_t* __restrict__ Are, const uint16_t* __restrict__ Aim,
    const uint16_t* __restrict__ Bre, const uint16_t* __restrict__ Bim, const uint16_t* __restrict__ Cre,
    const uint16_t* __restrict__ Cim, const uint16_t* __restrict__ logdt, const uint16_t* __restrict__ Dskip,
    char* __restrict__ ws, const char* __restrict__ outp, const char* __restrict__ w1p, const char* __restrict__ w2p) {
  extern __shared__ __attribute__((aligned(16))) char smem[];
  const int tid = threadIdx.x, lane = tid & 63;
  const int wv = __builtin_amdgcn_readfirstlane(tid >> 6);
  const int bid = blockIdx.x;
  STAMP_DECL
  const int l0 = bid * 8;
  if (wv == 8) {
    const unsigned dump = (unsigned)(size_t)(lds_char*)smem + K1_DUMP;
    {
      const char* pc;
      int o2, o3, o4;
      asm volatile("s_getpc_b64 %0\n\ts_mov_b32 %1, k2_conv@rel32@lo+4\n\ts_mov_b32 %2, k3_gemm1@rel32@lo+12\n\t"
                   "s_mov_b32 %3, k4_gemm2@rel32@lo+20" : "=s"(pc), "=s"(o2), "=s"(o3), "=s"(o4));
      const int o = (bid >> 3) * 512 + (lane & 31) * 16;
      glds16(pc + (lane < 32 ? o2 + min(o, CS2 - 16) : o4 + min(o, CS4 - 16)), dump);
      glds16(pc + (o3 + min(o, CS3 - 16)), dump);
    }
    if (bid < 8 && lane < 16) {
      const void* p = lane == 0 ? (const void*)Are : lane == 1 ? (const void*)Aim : lane == 2 ? (const void*)Bre
                    : lane == 3 ? (const void*)Bim : lane == 4 ? (const void*)Cre : lane == 5 ? (const void*)Cim
                    : lane == 6 ? (const void*)logdt : lane == 7 ? (const void*)Dskip : lane == 8 ? (const void*)w1p
                    : lane == 9 ? (const void*)w2p : lane == 10 ? (const void*)ws : lane == 11 ? (const void*)outp : (const void*)x;
      g_rec[bid].p[lane] = p;
    }
    const int dbase = 64 * (bid & 7) + 2 * (bid >> 3);
    const int po = dbase * N_ + (lane & 31) * 8;
    glds16(x + (size_t)((bid >> 3) * 64 + lane) * D_ + (bid & 7) * 64, dump);
    glds16((lane < 32 ? Are : Aim) + po, dump);
    glds16((lane < 32 ? Bre : Bim) + po, dump);
    glds16((lane < 32 ? Cre : Cim) + po, dump);
    glds16((lane < 32 ? logdt : Dskip) + (dbase & ~7), dump);
    return;
  }
  const u32x4 xr = *(const u32x4*)(x + (size_t)(l0 + wv) * D_ + lane * 8);
  STAMP(1);
  {
    f32x2 v[4];
#pragma unroll
    for (int j = 0; j < 4; ++j) v[j] = f32x2{bflo(xr[j]), bfhi(xr[j])};
    const f32x2 s2 = (v[0] + v[1]) + (v[2] + v[3]);
    const float mean = wave_sum(s2.x + s2.y) * (1.0f / D_);
    f32x2 q2 = f32x2{0.f, 0.f};
#pragma unroll
    for (int j = 0; j < 4; ++j) { const f32x2 dd = v[j] - mean; q2 = __builtin_elementwise_fma(dd, dd, q2); }
    const float var = wave_sum(q2.x + q2.y) * (1.0f / D_);
    const float m = rbf(mean);
    const float sd = rbf(__builtin_amdgcn_sqrtf(rbf(rbf(var) + 1e-5f)));
    if (lane == 0) *(f32x2*)(ws + WS_ST + (size_t)(l0 + wv) * 8) = f32x2{m, 1.0f / sd};
  }
  STAMP(3);
  STAMP_FLUSH(0);
}

constexpr int UC_S = 4160;
constexpr int UC_B4 = 4 * UC_S + 32;
constexpr int UREG = UC_B4 + 4 * UC_S;
constexpr int KOFF = UREG;
constexpr int T_OFF = KOFF + 512 + 4096;
constexpr int T_RS = 528;
constexpr int KF_OFF = T_OFF + 57 * T_RS;
constexpr int XL_OFF = KF_OFF + 3104;
constexpr int CH_LDS = XL_OFF + 4096;
constexpr int UL_OFF = 3 * UC_S + UC_B4;
constexpr int K2_CNT = 2 * CH_LDS + 1024;
constexpr int K2_LDS = K2_CNT + 64;

extern "C" __global__ __launch_bounds__(768) void k2_conv(char* __restrict__ ws_) {
  const uint16_t* __restrict__ Are = REC_PTR(const uint16_t*, 0);
  const uint16_t* __restrict__ Aim = REC_PTR(const uint16_t*, 1);
  const uint16_t* __restrict__ Bre = REC_PTR(const uint16_t*, 2);
  const uint16_t* __restrict__ Bim = REC_PTR(const uint16_t*, 3);
  const uint16_t* __restrict__ Cre = REC_PTR(const uint16_t*, 4);
  const uint16_t* __restrict__ Cim = REC_PTR(const uint16_t*, 5);
  const uint16_t* __restrict__ logdt = REC_PTR(const uint16_t*, 6);
  const uint16_t* __restrict__ Dskip = REC_PTR(const uint16_t*, 7);
  const uint16_t* __restrict__ w1 = REC_PTR(const uint16_t*, 8);
  const uint16_t* __restrict__ w2 = REC_PTR(const uint16_t*, 9);
  char* __restrict__ ws = REC_PTR(char*, 10);
  const uint16_t* __restrict__ x = REC_PTR(const uint16_t*, 12);
  extern __shared__ __attribute__((aligned(16))) char smem[];
  const int tid = threadIdx.x, lane = tid & 63;
  const int wv = __builtin_amdgcn_readfirstlane(tid >> 6);
  const int bid = blockIdx.x;
  STAMP_DECL
  const int m = lane & 15, kg = lane >> 4;
  const int dbase = 64 * (bid & 7) + 2 * (bid >> 3);
  const int chs = (wv >> 1) & 1, half = wv & 1;
  typedef __attribute__((address_space(3))) uint32_t lds_u32;
  if (wv >= 8) {
    const int i = tid - 512, w = wv - 8;
    uint32_t xv[8], xp;
    f32x4 sv[4];
    f32x2 sp;
    STAMP(1);
#define LDS4(j) asm volatile("global_load_dwordx4 %0, %1, %2 offset:" #j "*16" : "=v"(sv[j]) : "v"(i * 64u), "s"(ws + WS_ST))
    LDS4(0); LDS4(1); LDS4(2); LDS4(3);
    const unsigned rp = max(512 * w - 8 + min(lane, 7), 0);
    asm volatile("global_load_dwordx2 %0, %1, %2" : "=v"(sp) : "v"(rp * 8u), "s"(ws + WS_ST));
    if (w >= GBAR) RAW_BARRIER();
    for (int k = 0; k < (w >= GBAR ? w - GBAR : w); ++k) __builtin_amdgcn_s_sleep(GSTAG);
    if (w >= GBAR) __builtin_amdgcn_s_sleep(GST0);
    const unsigned xo = i * 8192u + dbase * 2u;
    asm volatile("global_load_dword %0, %1, %2" : "=v"(xp) : "v"(rp * 1024u + dbase * 2u), "s"(x));
#define LDX(j, o) asm volatile("global_load_dword %0, %1, %2 offset:" #o : "=v"(xv[j]) : "v"(xo + (j & 4) * 1024u), "s"(x))
    LDX(0, 0); LDX(1, 1024); LDX(2, 2048); LDX(3, 3072); LDX(4, 0); LDX(5, 1024); LDX(6, 2048); LDX(7, 3072);
    if (w < GBAR) RAW_BARRIER();
    STAMP(2);
    asm volatile("s_waitcnt vmcnt(8)" : "+v"(xp), "+v"(sp), "+v"(sv[0]), "+v"(sv[1]), "+v"(sv[2]), "+v"(sv[3]));
    auto pkh = [](float lo, float hi) { return __builtin_bit_cast(uint32_t, __builtin_amdgcn_cvt_pkrtz(lo, hi)); };
    uint32_t P0[4], P1[4];
    {
      const uint32_t a = cvt2(f32x2{bflo(xp), bfhi(xp)} - sp[0]);
      const uint32_t u = cvt2(f32x2{bflo(a), bfhi(a)} * sp[1]);
      const uint32_t h = w ? pkh(bflo(u), bfhi(u)) : 0u;
#pragma unroll
      for (int j = 0; j < 4; ++j) {
        const uint32_t e1 = __builtin_amdgcn_readlane(h, 7 - 2 * j), e0 = __builtin_amdgcn_readlane(h, 6 - 2 * j);
        P0[j] = (e1 & 0xffffu) | (e0 << 16);
        P1[j] = (e1 >> 16) | (e0 & 0xffff0000u);
      }
    }
    uint32_t S0[8], S1[8];
    u32x4 x0, x1;
    auto pair = [&](auto J) {
      constexpr int j = decltype(J)::value;
      asm volatile("s_waitcnt vmcnt(%2)" : "+v"(xv[2 * j]), "+v"(xv[2 * j + 1]) : "i"(6 - 2 * j));
      uint32_t ub[2];
#pragma unroll
      for (int k = 0; k < 2; ++k) {
        const uint32_t a = cvt2(f32x2{bflo(xv[2 * j + k]), bfhi(xv[2 * j + k])} - sv[j][2 * k]);
        ub[k] = cvt2(f32x2{bflo(a), bfhi(a)} * sv[j][2 * k + 1]);
      }
      S0[3 - j] = pkh(bflo(ub[1]), bflo(ub[0]));
      S1[3 - j] = pkh(bfhi(ub[1]), bfhi(ub[0]));
      x0[j] = __builtin_amdgcn_perm(xv[2 * j + 1], xv[2 * j], 0x05040100u);
      x1[j] = __builtin_amdgcn_perm(xv[2 * j + 1], xv[2 * j], 0x07060302u);
      S0[7 - j] = __builtin_amdgcn_update_dpp(P0[3 - j], S0[3 - j], 0x138, 0xf, 0xf, false);
      S1[7 - j] = __builtin_amdgcn_update_dpp(P1[3 - j], S1[3 - j], 0x138, 0xf, 0xf, false);
      asm volatile("" : "+v"(S0[7 - j]), "+v"(S1[7 - j]), "+v"(x0[j]), "+v"(x1[j]));
    };
    pair(std::integral_constant<int, 0>{});
    pair(std::integral_constant<int, 1>{});
    pair(std::integral_constant<int, 2>{});
    pair(std::integral_constant<int, 3>{});
    STAMP(3);
    char* uc = smem + 4080 - 16 * i;
#pragma unroll
    for (int ml = 7; ml >= 0; --ml) {
      const int s = 7 - ml;
      u32x4 o0, o1;
#pragma unroll
      for (int k = 0; k < 4; ++k) {
        o0[k] = (s & 1) ? __builtin_amdgcn_alignbit(S0[(s >> 1) + k + 1], S0[(s >> 1) + k], 16) : S0[(s >> 1) + k];
        o1[k] = (s & 1) ? __builtin_amdgcn_alignbit(S1[(s >> 1) + k + 1], S1[(s >> 1) + k], 16) : S1[(s >> 1) + k];
      }
      *(u32x4*)(uc + (ml & 3) * UC_S + (ml >> 2) * UC_B4) = o0;
      *(u32x4*)(uc + CH_LDS + (ml & 3) * UC_S + (ml >> 2) * UC_B4) = o1;
    }
    *(u32x4*)(smem + XL_OFF + 16 * i) = x0;
    *(u32x4*)(smem + CH_LDS + XL_OFF + 16 * i) = x1;
    WAIT_LGKM0();
    STAMP(4);
    RAW_BARRIER();
    {
      const char* src = (const char*)w1 + (size_t)(bid >> 3) * 16 * (F_ * 2) + (bid & 1) * F_;
      const unsigned dump = (unsigned)(size_t)(lds_char*)smem + 2 * CH_LDS;
#pragma unroll
      for (int j = 0; j < 7; ++j) {
        const unsigned c = (w * 7 + j) * 64 + lane, r = c / 112u;
        glds16(src + r * (F_ * 2) + (c - r * 112u) * 16u, dump);
      }
#pragma unroll
      for (int j = 0; j < 2; ++j)
        glds16((const char*)w2 + (size_t)bid * 7168 + min(w * 2 + j, 6) * 1024 + lane * 16, dump);
    }
    WAIT_VM(0);
    return;
  }
  uint32_t pds;
  int sexp;
  {
    const int ch = wv >> 2, wq = wv & 3, part = wq >> 1;
    const int t8 = tid & 255, n = tid & 127;
    const int d = dbase + ch;
    char* cb = smem + ch * CH_LDS;
    char* tb = cb + T_OFF;
    float* Kf = (float*)(cb + KF_OFF);
    uint32_t pAr, pAi, pBr, pBi, pCr, pCi, pdt;
    {
      const unsigned po = (d * N_ + n) * 2u;
#define LD16(r, o, p) asm volatile("global_load_ushort %0, %1, %2" : "=v"(r) : "v"(o), "s"(p))
      LD16(pdt, d * 2u, logdt); LD16(pAr, po, Are); LD16(pAi, po, Aim); LD16(pBr, po, Bre); LD16(pBi, po, Bim);
      LD16(pCr, po, Cre); LD16(pCi, po, Cim); LD16(pds, (dbase + chs) * 2u, Dskip);
    }
    lds_u32* cnt = (lds_u32*)(smem + K2_CNT) + 4 * ch;
    auto gsync = [&](int ph) {
      asm volatile("" ::: "memory");
      if (lane == 0) __hip_atomic_fetch_add(cnt + ph, 1u, __ATOMIC_RELAXED, __HIP_MEMORY_SCOPE_WORKGROUP);
      while (__builtin_amdgcn_readfirstlane(*(volatile lds_u32*)(cnt + ph)) < 4u) __builtin_amdgcn_s_sleep(1);
      asm volatile("" ::: "memory");
    };
    if (t8 >= 64 && t8 < 68) cnt[t8 - 64] = 0u;
    if (t8 < 32) *(u32x4*)(cb + KOFF + t8 * 16) = u32x4{0, 0, 0, 0};
    else if (t8 < 40) {
      const int j = t8 - 32;
      *(u32x4*)(cb + (j & 3) * UC_S + (j >> 2) * UC_B4 + 256 * 16) = u32x4{0, 0, 0, 0};
    }
    asm volatile("s_waitcnt vmcnt(0)" : "+v"(pdt), "+v"(pAr), "+v"(pAi), "+v"(pBr), "+v"(pBi), "+v"(pCr), "+v"(pCi), "+v"(pds));
    const float dt = rbf(expf(bflo(pdt)));
    const float zx = dt * bflo(pAr);
    const float zy = dt * bflo(pAi);
    f32x2 c;
    {
      const float br = dt * bflo(pBr), bi = dt * bflo(pBi);
      const float cr = bflo(pCr), ci = bflo(pCi);
      c = f32x2{cr * br - ci * bi, -(cr * bi + ci * br)};
    }
    {
      const float mg = wave_max(fmaxf(fabsf(c.x), fabsf(c.y)) * fmaxf(1.f, __expf(2048.f * zx)));
      if (lane == 0 && part == 0) Kf[772 + (wq & 1)] = mg;
    }
    auto putn = [&](auto NV, const int* rows, const f32x2* v) {
      constexpr int nv = decltype(NV)::value;
      uint32_t H[nv];
#pragma unroll
      for (int i = 0; i < nv; ++i) H[i] = cvt2h(v[i]);
#pragma unroll
      for (int i = 0; i < nv; ++i) *(uint32_t*)(tb + rows[i] * T_RS + 4 * n) = H[i];
    };
    auto cmuln = [&](auto NV, f32x2* o, const f32x2* v, f32x2 mm) {
      constexpr int nv = decltype(NV)::value;
      const f32x2 mn = f32x2{-mm.y, mm.x};
      f32x2 t[nv];
#pragma unroll
      for (int i = 0; i < nv; ++i) t[i] = f32x2{v[i].x, v[i].x} * mm;
#pragma unroll
      for (int i = 0; i < nv; ++i) o[i] = __builtin_elementwise_fma(f32x2{v[i].y, v[i].y}, mn, t[i]);
    };
    using I4 = std::integral_constant<int, 4>;
    using I8 = std::integral_constant<int, 8>;
    using I6 = std::integral_constant<int, 6>;
    using I3 = std::integral_constant<int, 3>;
    STAMP(1);
    const f32x2 g1 = cexp_(128.f * zx, -128.f * zy);
    const f32x2 ea = cexp_(part ? 2.f * zx : zx, part ? -2.f * zy : -zy);
    WAIT_LGKM0();
    RAW_BARRIER();
    {
      const f32x2 m2 = *(const f32x2*)(Kf + 772);
      const int ex = (int)((__float_as_uint(fmaxf(m2.x, m2.y)) >> 23) & 255u) - 127;
      sexp = 13 - ex;
      sexp = sexp > 126 ? 126 : (sexp < -126 ? -126 : sexp);
      c *= __uint_as_float((uint32_t)(127 + sexp) << 23);
    }
    STAMP(2);
    if (part == 0) {
      const f32x2 e1 = ea;
      const f32x2 e8 = cexp_(8.f * zx, 8.f * zy);
      const f32x2 e2 = cmul(e1, e1), e16 = cmul(e8, e8);
      const f32x2 e4 = cmul(e2, e2), e32 = cmul(e16, e16);
      f32x2 A[8], Bv[4];
      A[0] = c; A[1] = cmul(c, e1); A[2] = cmul(c, e2);
      Bv[0] = f32x2{1.f, 0.f}; Bv[1] = e8; Bv[2] = e16; Bv[3] = cmul(e16, e8);
      A[3] = cmul(A[1], e2);
      cmuln(I4{}, A + 4, A, e4);
      { const int r[4] = {41, 42, 43, 44}; putn(I4{}, r, Bv); }
      cmuln(I4{}, Bv, Bv, e32);
      { const int r[8] = {0, 1, 2, 3, 4, 5, 6, 7}; putn(I8{}, r, A); }
      cmuln(I8{}, A, A, g1);
      { const int r[4] = {45, 46, 47, 48}; putn(I4{}, r, Bv); }
      cmuln(I4{}, Bv, Bv, e32);
      { const int r[8] = {8, 9, 10, 11, 12, 13, 14, 15}; putn(I8{}, r, A); }
      { const int r[4] = {49, 50, 51, 52}; putn(I4{}, r, Bv); }
      cmuln(I4{}, Bv, Bv, e32);
      { const int r[4] = {53, 54, 55, 56}; putn(I4{}, r, Bv); }
    } else {
      const f32x2 e2 = ea;
      const f32x2 g2 = cmul(g1, g1), e4 = cmul(e2, e2);
      const f32x2 g4 = cmul(g2, g2);
      f32x2 Q[8], R[6], S[4];
      Q[0] = cmul(c, g2);
      Q[1] = cmul(Q[0], g1); Q[2] = cmul(Q[0], g2);
      Q[3] = cmul(Q[2], g1);
      cmuln(I4{}, Q + 4, Q, g4);
      cmuln(I6{}, R, Q, e4);
      { f32x2 t[4] = {Q[0], R[0], Q[1], R[1]}; cmuln(I4{}, S, t, e2); }
      { const int r[8] = {16, 20, 24, 26, 28, 30, 32, 33}; putn(I8{}, r, Q); }
      cmuln(I4{}, Q, Q + 4, g4);
      { const int r[6] = {18, 22, 25, 27, 29, 31}; putn(I6{}, r, R); }
      cmuln(I3{}, Q + 4, Q, g4);
      { const int r[4] = {17, 19, 21, 23}; putn(I4{}, r, S); }
      { const int r[4] = {34, 35, 36, 37}; putn(I4{}, r, Q); }
      { const int r[3] = {38, 39, 40}; putn(I3{}, r, Q + 4); }
    }
    STAMP(3);
    gsync(1);
    STAMP(4);
    if (wq < 3) {
      const char* ta = tb + (16 * wq + m) * T_RS + kg * 16;
      const char* tq = tb + (41 + m) * T_RS + kg * 16;
      f32x4 a1 = f32x4{0, 0, 0, 0}, a2 = f32x4{0, 0, 0, 0};
      f16x8 Ah[8], Bh[8];
#pragma unroll
      for (int s = 0; s < 8; ++s) { Ah[s] = *(const f16x8*)(ta + 64 * s); Bh[s] = *(const f16x8*)(tq + 64 * s); }
      __builtin_amdgcn_sched_barrier(0);
#pragma unroll
      for (int s = 0; s < 8; s += 2) {
        a1 = __builtin_amdgcn_mfma_f32_16x16x32_f16(Ah[s], Bh[s], a1, 0, 0, 0);
        a2 = __builtin_amdgcn_mfma_f32_16x16x32_f16(Ah[s + 1], Bh[s + 1], a2, 0, 0, 0);
      }
      a1 += a2;
      float mx = 0.f;
#pragma unroll
      for (int j = 0; j < 4; ++j) {
        const int ix = (16 * wq + 4 * kg + j) * 16 + m;
        Kf[ix] = a1[j];
        if (ix <= 640) mx = fmaxf(mx, fabsf(a1[j]));
      }
      mx = wave_max(mx);
      if (lane == 0) Kf[768 + wq] = mx;
    }
    STAMP(5);
    gsync(2);
    STAMP(6);
    {
      const int a = t8 >> 4, b = t8 & 15;
      const float* kp = Kf + k_rowbase(a) * 16 + b;
      float kv[8];
#pragma unroll
      for (int i = 0; i < 8; ++i) kv[i] = kp[16 * i];
      float vn = Kf[k_rowbase((t8 + 1) >> 4) * 16 + ((t8 + 1) & 15)];
      const f32x4 m4 = *(const f32x4*)(Kf + 768);
      __builtin_amdgcn_sched_barrier(0);
      const float mx = fmaxf(fmaxf(m4[0], m4[1]), m4[2]);
      const int ex = (int)((__float_as_uint(mx) >> 23) & 255u) - 127;
      int sh = 14 - ex;
      sh = sh > 126 ? 126 : (sh < -126 ? -126 : sh);
      const float scale = __uint_as_float((uint32_t)(127 + sh) << 23);
      if (t8 == 0) {
        int e = -(sh + sexp);
        e = e > 126 ? 126 : (e < -126 ? -126 : e);
        Kf[774] = __uint_as_float((uint32_t)(127 + e) << 23);
      }
      float v[8];
      {
        const bool c2 = a < 2, c4 = a < 4, c8 = a < 8;
        const float w0 = kv[0], w1_ = kv[1], w2_ = kv[2], w3 = kv[3];
        v[0] = w0;
        v[1] = c2 ? w1_ : w0;
        v[2] = c2 ? w2_ : c4 ? w1_ : w0;
        v[3] = c2 ? w3 : c4 ? w2_ : c8 ? w1_ : w0;
        v[4] = c2 ? kv[4] : c4 ? w2_ : c8 ? w1_ : ((t8 & 1) ? vn : w0);
        v[5] = c2 ? kv[5] : c4 ? w2_ : c8 ? w1_ : vn;
        v[6] = c2 ? kv[6] : c4 ? w3 : vn;
        v[7] = c2 ? kv[7] : vn;
#pragma unroll
        for (int i = 0; i < 8; ++i) v[i] *= scale;
      }
      const u32x4 kk = u32x4{pack2h(v[0], v[1]), pack2h(v[2], v[3]), pack2h(v[4], v[5]), pack2h(v[6], v[7])};
      *(u32x4*)(cb + KOFF + 512 + t8 * 16) = kk;
    }
    __builtin_amdgcn_sched_barrier(0);
    STAMP(7);
  }
  __syncthreads();
  STAMP(8);
  if (wv >= 4) return;
  u32x2 xe[4], ue[4];
#pragma unroll
  for (int j = 0; j < 4; ++j) {
    const int t0 = 256 * (4 * half + j) + 16 * m + 4 * kg;
    xe[j] = *(const u32x2*)(smem + chs * CH_LDS + XL_OFF + 2 * t0);
    ue[j] = *(const u32x2*)(smem + chs * CH_LDS + UL_OFF + 2 * (2044 - t0));
  }

  char* cb = smem + chs * CH_LDS;
  const int ml = m & 7, mh = m >> 3;
  const char* abase = cb + (ml & 3) * UC_S + (ml >> 2) * UC_B4 + 16 * (253 - mh + kg) - 256 * half;
  const char* bbase = cb + KOFF + 512 + 32 * m + 16 * kg - 32 - 256 * half;
  f32x4 acc[8];
#pragma unroll
  for (int t = 0; t < 8; ++t) acc[t] = f32x4{0, 0, 0, 0};
  f16x8 af[2][8], bfr[2][8];
  af[0][0] = *(const f16x8*)(abase);
#pragma unroll
  for (int j = 0; j < 8; ++j) bfr[0][j] = *(const f16x8*)(bbase + 512 * j);
#pragma unroll
  for (int j = 1; j < 8; ++j) af[0][j] = *(const f16x8*)(abase - 512 * j);
  const float kinv = ((const float*)(cb + KF_OFF))[774];
  uint32_t ud[4][2];
  {
    const float dsk = bflo(pds);
#pragma unroll
    for (int j = 0; j < 4; ++j)
#pragma unroll
      for (int r2 = 0; r2 < 2; ++r2) {
        const uint32_t uw = ue[j][1 - r2];
        const f16x2 u2 = __builtin_bit_cast(f16x2, uw);
        ud[j][r2] = cvt2(f32x2{(float)u2[1], (float)u2[0]} * dsk);
        asm volatile("" : "+v"(ud[j][r2]));
      }
  }
  __builtin_amdgcn_sched_barrier(0);
#pragma unroll
  for (int s = 0; s < 4; ++s) {
    int slot = 0;
#pragma unroll
    for (int al = 0; al < 8; ++al)
#pragma unroll
      for (int T = al; T < 8; ++T) {
        acc[T] = __builtin_amdgcn_mfma_f32_16x16x32_f16(af[s & 1][al], bfr[s & 1][T - al], acc[T], 0, 0, 0);
        __builtin_amdgcn_sched_barrier(0);
        if (s < 3 && (slot & 1) == 0 && slot < 32) {
          const int j = slot >> 1;
          if (j >= 1 && j <= 8) bfr[(s + 1) & 1][j - 1] = *(const f16x8*)(bbase + 512 * (j - 1) - 64 * (s + 1));
          else { const int ja = j == 0 ? 0 : j - 8; af[(s + 1) & 1][ja] = *(const f16x8*)(abase - 512 * ja - 64 * (s + 1)); }
          __builtin_amdgcn_sched_barrier(0);
        }
        ++slot;
      }
  }
  STAMP(9);
  char* xb = cb + T_OFF;
  f32x4 keep[4];
  if (half) {
#pragma unroll
    for (int j = 0; j < 4; ++j) { *(f32x4*)(xb + 4096 + j * 1024 + lane * 16) = acc[j]; keep[j] = acc[4 + j]; }
    asm volatile("" ::: "memory");
  } else {
#pragma unroll
    for (int j = 0; j < 4; ++j) { *(f32x4*)(xb + j * 1024 + lane * 16) = acc[4 + j]; keep[j] = acc[j]; }
    asm volatile("" ::: "memory");
  }
  {
    lds_u32* cn = (lds_u32*)(smem + K2_CNT) + 4 * chs + 3;
    if (lane == 0) __hip_atomic_fetch_add(cn, 1u, __ATOMIC_RELAXED, __HIP_MEMORY_SCOPE_WORKGROUP);
    while (__builtin_amdgcn_readfirstlane(*(volatile lds_u32*)cn) < 2u) __builtin_amdgcn_s_sleep(0);
    asm volatile("" ::: "memory");
  }
  STAMP(10);
  uint16_t* hT = (uint16_t*)(ws + WS_HT) + (size_t)(dbase + chs) * L_;
#pragma unroll
  for (int j = 0; j < 4; ++j) {
    const f32x4 o = *(const f32x4*)(xb + (1 - half) * 4096 + j * 1024 + lane * 16);
    const f32x4 a = keep[j];
    u32x2 hv;
#pragma unroll
    for (int r2 = 0; r2 < 2; ++r2) {
      const uint32_t y = cvt2((f32x2{a[2 * r2], a[2 * r2 + 1]} + f32x2{o[2 * r2], o[2 * r2 + 1]}) * kinv);
      const uint32_t t = cvt2(f32x2{bflo(y), bfhi(y)} + f32x2{bflo(ud[j][r2]), bfhi(ud[j][r2])});
      hv[r2] = cvt2(f32x2{bflo(t), bfhi(t)} + f32x2{bflo(xe[j][r2]), bfhi(xe[j][r2])});
    }
    *(u32x2*)(hT + 256 * (4 * half + j) + 16 * m + 4 * kg) = hv;
  }
  STAMP(11);
  STAMP_FLUSH(1);
}

constexpr int G1_HS = 64 * 256;
constexpr int G1_WS = 64 * 224;
constexpr int G1_STAGE = G1_HS + G1_WS;
constexpr int G1_NS = 5;
constexpr int G1_ST = G1_NS * G1_STAGE;
constexpr int G1_DUMP = G1_ST + 2048;
constexpr int K3_LDS = G1_DUMP + 1024;
#define G1_LA 2
#define PF2_A 2
__device__ constexpr int g1_pfs(int kt) {
  return kt <= -1 ? 0 : kt <= 5 ? PF2_A * (kt + 1) : kt == 6 ? PF2_A * 6 + (28 - PF2_A * 6) / 2 : 28;
}

__device__ __forceinline__ bf16x4 tr_read(const char* p) {
  return __builtin_amdgcn_ds_read_tr16_b64_v4i16((lds_bf16x4*)p);
}
__device__ __forceinline__ bf16x8 cat8(bf16x4 a, bf16x4 b) {
  return bf16x8{a[0], a[1], a[2], a[3], b[0], b[1], b[2], b[3]};
}
__device__ __forceinline__ void swap16(uint32_t& a, uint32_t& b) {
  auto r = __builtin_amdgcn_permlane16_swap(a, b, false, false);
  a = r[0]; b = r[1];
}
__device__ __forceinline__ float sum_g(float t) {
  { const auto r = __builtin_amdgcn_permlane16_swap(__float_as_uint(t), __float_as_uint(t), false, false);
    t = __uint_as_float(r[0]) + __uint_as_float(r[1]); }
  { const auto r = __builtin_amdgcn_permlane32_swap(__float_as_uint(t), __float_as_uint(t), false, false);
    t = __uint_as_float(r[0]) + __uint_as_float(r[1]); }
  return t;
}
#define WAIT_VM_N(n) asm volatile("s_waitcnt vmcnt(%0)" :: "i"(n) : "memory")
__device__ __forceinline__ void glds16s(unsigned voff, const void* sbase, unsigned lds_dst) {
  asm volatile("s_mov_b32 m0, %2\n\ts_nop 0\n\tglobal_load_lds_dwordx4 %0, %1" :: "v"(voff), "s"(sbase), "s"(lds_dst)
               : "memory");
}

template <int WF>
__device__ __forceinline__ void k3_body(char* __restrict__ ws, char* smem,
                                        const int lane, const int wv, const int l0, const int f0, const int ft) {
  constexpr int NT = WF ? 3 : 4;
  constexpr int CI0 = WF ? 4 : 0;
  STAMP_DECL
  const int wl = wv & 3;
  const unsigned lbase = (unsigned)(size_t)(lds_char*)smem;
  const int g = lane >> 4, q = (lane >> 2) & 3, pp = lane & 3, r15 = lane & 15;
  int tix[NT];
#pragma unroll
  for (int i = 0; i < NT; ++i) {
    int t = i + (WF ? (wl == 3 ? 0 : wl) : wl);
    t = t >= NT ? t - NT : t;
    tix[i] = CI0 + t;
  }
  const int xv = 2 * (4 * (g & 1) + q);
  unsigned hb[3][2], wb[3][NT];
#pragma unroll
  for (int r = 0; r < 3; ++r) {
#pragma unroll
    for (int cc = 0; cc < 2; ++cc) {
      hb[r][cc] = lbase + r * 2 * G1_STAGE + (4 * g + q) * 256 + ((((2 * (2 * wl + cc)) ^ xv) + (pp >> 1)) * 16) + 8 * (pp & 1);
      asm volatile("" : "+v"(hb[r][cc]));
    }
#pragma unroll
    for (int i = 0; i < NT; ++i) {
      wb[r][i] = lbase + r * 2 * G1_STAGE + G1_HS + (4 * g + q) * 224 + 8 * pp + 32 * tix[i];
      asm volatile("" : "+v"(wb[r][i]));
    }
  }
  const int lrow = l0 + 32 * wl + r15;

  f32x4 acc[NT][2];
#pragma unroll
  for (int i = 0; i < NT; ++i) { acc[i][0] = f32x4{0, 0, 0, 0}; acc[i][1] = f32x4{0, 0, 0, 0}; }
  f32x4 accS[2], accG[2], accC;
  accS[0] = accS[1] = accG[0] = accG[1] = accC = f32x4{0, 0, 0, 0};
  bf16x8 ones = {0x3f80, 0x3f80, 0x3f80, 0x3f80, 0x3f80, 0x3f80, 0x3f80, 0x3f80};
  asm volatile("" : "+v"(ones));

  struct Frag { bf16x8 h[2], w[NT]; };
  auto trl = [&](unsigned a) { return __builtin_amdgcn_ds_read_tr16_b64_v4i16((lds_bf16x4*)(size_t)a); };
  auto ld1 = [&](Frag& F, int kt, int ks, int i) {
    const int bf = kt % G1_NS, r = bf >> 1, o = (bf & 1) * G1_STAGE;
    if (i < 2) F.h[i] = cat8(trl(hb[r][i] + o + ks * 8192), trl(hb[r][i] + o + ks * 8192 + 4096));
    else F.w[i - 2] = cat8(trl(wb[r][i - 2] + o + ks * 32 * 224), trl(wb[r][i - 2] + o + (ks * 32 + 16) * 224));
  };
  constexpr int NM = 2 * NT + 1 + (WF ? 4 : 0);
  auto mm1 = [&](const Frag& F, int i) {
    if (i < 2 * NT) {
      const int ci = i >> 1, cc = i & 1;
      acc[ci][cc] = __builtin_amdgcn_mfma_f32_16x16x32_bf16(F.w[ci], F.h[cc], acc[ci][cc], 0, 0, 0);
    } else if (i == 2 * NT) {
      accC = __builtin_amdgcn_mfma_f32_16x16x32_bf16(F.w[0], ones, accC, 0, 0, 0);
    } else {
      const int cc = i & 1;
      if (i < 2 * NT + 3) accS[cc] = __builtin_amdgcn_mfma_f32_16x16x32_bf16(ones, F.h[cc], accS[cc], 0, 0, 0);
      else accG[cc] = __builtin_amdgcn_mfma_f32_16x16x32_bf16(F.h[cc], F.h[cc], accG[cc], 0, 0, 0);
    }
  };
  auto step = [&](const Frag& C, Frag& N, int nkt, int nks, int kstep) {
#pragma unroll
    for (int i = 0; i < NM; ++i) {
      mm1(C, kstep == 15 ? NM - 1 - i : i);
      __builtin_amdgcn_sched_barrier(0);
      if (nkt >= 0 && i < NT + 2) ld1(N, nkt, nks, i);
      __builtin_amdgcn_sched_barrier(0);
    }
    if (!WF && kstep < 2) {
      if (kstep == (ft & 1)) {
        asm volatile("" ::: "memory");
#pragma unroll
        for (int cc = 0; cc < 2; ++cc) {
          const u32x4 hv = __builtin_bit_cast(u32x4, C.h[cc]);
          uint32_t a0 = hv[0], a1 = hv[1], b0 = hv[2], b1 = hv[3];
          swap16(a0, b0); swap16(a1, b1);
          uint16_t* p = (uint16_t*)(ws + WS_HR) + (size_t)(lrow + 16 * cc) * D_ + 32 * ft + 16 * (g & 1) + 8 * (g >> 1);
          *(u32x4*)p = u32x4{a0, a1, b0, b1};
        }
      }
      __builtin_amdgcn_sched_barrier(0);
    }
  };
  Frag F0, F1;
  STAMP(1);
  RAW_BARRIER();
  STAMP(2);
#pragma unroll
  for (int i = 0; i < NT + 2; ++i) ld1(F0, 0, 0, i);
#pragma unroll
  for (int kt = 0; kt < 8; ++kt) {
    STAMP(3 + kt);
    step(F0, F1, kt, 1, 2 * kt);
    if (kt < 7) {
      if (G1_LA == G1_NS - 1 && kt + G1_NS < 8) WAIT_LGKM0();
      RAW_BARRIER();
    }
    step(F1, F0, kt < 7 ? kt + 1 : -1, 0, 2 * kt + 1);
  }
  STAMP(11);
  float* stt = (float*)(smem + G1_ST);
  float mm[2], rs[2];
  if (r15 == 0 && (!WF || wl < 3)) *(f32x4*)(stt + 256 + 16 * tix[0] + 4 * g) = accC;
  if (WF) {
    f32x2 sq, mean;
#pragma unroll
    for (int cc = 0; cc < 2; ++cc) {
      sq[cc] = (pp == 0) ? accG[cc][0] : (pp == 1) ? accG[cc][1] : (pp == 2) ? accG[cc][2] : accG[cc][3];
      mean[cc] = accS[cc][0];
    }
    mean *= (1.0f / D_);
    f32x2 var = sq * (1.0f / D_) - mean * mean;
    var = f32x2{fmaxf(var[0], 0.f), fmaxf(var[1], 0.f)};
    const uint32_t mb = cvt2(mean);
    uint32_t t = cvt2(var);
    t = cvt2(f32x2{bflo(t), bfhi(t)} + 1e-5f);
    t = cvt2(f32x2{__builtin_amdgcn_sqrtf(bflo(t)), __builtin_amdgcn_sqrtf(bfhi(t))});
    const float r0 = __builtin_amdgcn_rcpf(bflo(t)), r1 = __builtin_amdgcn_rcpf(bfhi(t));
    if (g == q) {
      *(f32x2*)(stt + 2 * (32 * wl + r15)) = f32x2{bflo(mb), r0};
      *(f32x2*)(stt + 2 * (32 * wl + 16 + r15)) = f32x2{bfhi(mb), r1};
    }
  }
  WAIT_LGKM0();
  RAW_BARRIER();
  __builtin_amdgcn_sched_barrier(0);
  f32x4 c4[NT];
#pragma unroll
  for (int ci = 0; ci < NT; ++ci) c4[ci] = *(const f32x4*)(stt + 256 + 16 * tix[ci] + 4 * g);
#pragma unroll
  for (int cc = 0; cc < 2; ++cc) {
    const f32x2 t = *(const f32x2*)(stt + 2 * (32 * wl + 16 * cc + r15));
    mm[cc] = t[0]; rs[cc] = t[1];
  }
  STAMP(12);
  uint16_t* H1 = (uint16_t*)(ws + WS_H1);
#pragma unroll
  for (int ci = 0; ci < NT; ++ci) {
    uint32_t pk[2][2];
#pragma unroll
    for (int cc = 0; cc < 2; ++cc)
#pragma unroll
      for (int j2 = 0; j2 < 2; ++j2) {
        const f32x2 a = {acc[ci][cc][2 * j2], acc[ci][cc][2 * j2 + 1]};
        const f32x2 c = {c4[ci][2 * j2], c4[ci][2 * j2 + 1]};
        const uint32_t vb = cvt2((a - mm[cc] * c) * rs[cc]);
        const f32x2 v = {bflo(vb), bfhi(vb)};
        const f32x2 e = v * -1.4426950408889634f;
        const f32x2 dn = f32x2{__builtin_amdgcn_exp2f(e[0]), __builtin_amdgcn_exp2f(e[1])} + 1.0f;
        const f32x2 o = v * f32x2{__builtin_amdgcn_rcpf(dn[0]), __builtin_amdgcn_rcpf(dn[1])};
        pk[cc][j2] = cvt2(o);
      }
    swap16(pk[0][0], pk[1][0]);
    swap16(pk[0][1], pk[1][1]);
    uint16_t* p = H1 + (size_t)(lrow + 16 * (g & 1)) * F_ + f0 + 16 * tix[ci] + 8 * (g >> 1);
    st16<SM3>(p, u32x4{pk[0][0], pk[0][1], pk[1][0], pk[1][1]});
    __builtin_amdgcn_sched_barrier(0);
  }
  STAMP(13);
  STAMP_FLUSH(2);
}

extern "C" __global__ __launch_bounds__(640) void k3_gemm1(char* __restrict__ ws_) {
  const uint16_t* __restrict__ w1 = REC_PTR(const uint16_t*, 8);
  const uint16_t* __restrict__ w2 = REC_PTR(const uint16_t*, 9);
  char* __restrict__ ws = REC_PTR(char*, 10);
  extern __shared__ __attribute__((aligned(16))) char smem[];
  const int tid = threadIdx.x, lane = tid & 63;
  const int wv = __builtin_amdgcn_readfirstlane(tid >> 6);
  const int bid = blockIdx.x;
  const int xcd = bid & 7, idx = bid >> 3;
  const int lt = 4 * (xcd >> 1) + (idx & 3), ft = 8 * (xcd & 1) + (idx >> 2);
  const int l0 = lt * 128, f0 = ft * 112;
  if (wv >= 8) {
    const unsigned lbase = (unsigned)(size_t)(lds_char*)smem;
    const uint16_t* hT = (const uint16_t*)(ws + WS_HT);
    const int kt0 = ft >> 1;
    const int lw = wv - 8;
    unsigned hoff[8], woff[7];
#pragma unroll
    for (int i = 0; i < 8; ++i) {
      const int row = (8 * lw + i) * 4 + (lane >> 4);
      hoff[i] = 2u * (row * L_ + l0 + 8 * ((lane & 15) ^ (2 * (row & 7))));
    }
#pragma unroll
    for (int i = 0; i < 7; ++i) {
      const int nn = (7 * lw + i) * 64 + lane;
      const int r2 = nn / 14, ch = nn - r2 * 14;
      woff[i] = 2u * (r2 * F_ + f0 + 8 * ch);
    }
    auto stage = [&](int kt) {
      const unsigned sb = lbase + (kt % G1_NS) * G1_STAGE;
      const int ka = (kt0 + kt) & 7;
      const uint16_t* hs = hT + (size_t)ka * 64 * L_;
      const uint16_t* wsrc = w1 + (size_t)ka * 64 * F_;
#pragma unroll
      for (int i = 0; i < 8; ++i) {
        glds16s(hoff[i], hs, sb + (8 * lw + i) * 1024);
        if (i < 7) glds16s(woff[i], wsrc, sb + G1_HS + (7 * lw + i) * 1024);
      }
    };
    const char* src = (const char*)w2 + (size_t)idx * 57344 + lw * 28672;
#pragma unroll
    for (int s = 0; s < G1_LA; ++s) stage(s);
#pragma unroll
    for (int kt = -1; kt < 7; ++kt) {
      WAIT_VM_N(((kt + G1_LA < 7 ? kt + G1_LA : 7) - kt - 1) * 15 + g1_pfs(kt) - g1_pfs(kt - G1_LA));
      RAW_BARRIER();
      if (kt + 1 + G1_LA < 8) stage(kt + 1 + G1_LA);
#pragma unroll
      for (int j = g1_pfs(kt); j < g1_pfs(kt + 1); ++j) glds16s(lane * 16u + j * 1024u, src, lbase + G1_DUMP);
    }
    RAW_BARRIER();
    WAIT_VM(0);
  } else if (wv < 4) k3_body<0>(ws, smem, lane, wv, l0, f0, ft);
  else k3_body<1>(ws, smem, lane, wv, l0, f0, ft);
}

constexpr int G2_HS = 64 * 256;
constexpr int G2_WS = 128 * 128;
constexpr int G2_STAGE = G2_HS + G2_WS;
constexpr int G2_NS = 5;
constexpr int G2_NK = F_ / 128;
constexpr int K4_LDS = G2_NS * G2_STAGE;

#define NLD 4
#define G2_LA 2
extern "C" __global__ __launch_bounds__(256 + 64 * NLD) void k4_gemm2(char* __restrict__ ws_) {
  const uint16_t* __restrict__ w2 = REC_PTR(const uint16_t*, 9);
  char* __restrict__ ws = REC_PTR(char*, 10);
  uint16_t* __restrict__ out = REC_PTR(uint16_t*, 11);
  extern __shared__ __attribute__((aligned(16))) char smem[];
  const int tid = threadIdx.x, lane = tid & 63;
  const int wv = __builtin_amdgcn_readfirstlane(tid >> 6);
  const int bid = blockIdx.x;
  const int xcd = bid & 7, idx = bid >> 3;
  STAMP_DECL
  const int lt = 4 * xcd + (idx & 3), dtile = idx >> 2;
  const int l0 = lt * 64, d0 = dtile * 64;
  const unsigned lbase = (unsigned)(size_t)(lds_char*)smem;
  const uint16_t* H1 = (const uint16_t*)(ws + WS_H1);
  if (wv >= 4) {
    constexpr int PP = 16 / NLD;
    const int p0 = (wv - 4) * PP;
    unsigned hoff[PP], woff[PP];
#pragma unroll
    for (int i = 0; i < PP; ++i) {
      const int row = (p0 + i) * 4 + (lane >> 4);
      hoff[i] = 2u * ((l0 + row) * F_ + 8 * ((lane & 15) ^ (row & 15)));
      const int r2 = (p0 + i) * 8 + (lane >> 3);
      const int gf = ((r2 >> 1) & 1) | (((r2 >> 3) & 1) << 1);
      woff[i] = 2u * (r2 * D_ + d0 + 8 * ((lane & 7) ^ (2 * gf)));
    }
    auto stage = [&](int kt) {
      const unsigned sb = lbase + (kt % G2_NS) * G2_STAGE + p0 * 1024;
#pragma unroll
      for (int i = 0; i < PP; ++i) {
        glds16s(woff[i], w2 + (size_t)kt * 128 * D_, sb + G2_HS + i * 1024);
        glds16s(hoff[i], H1 + kt * 128, sb + i * 1024);
      }
    };
#pragma unroll
    for (int s = 0; s < G2_LA; ++s) stage(s);
#pragma unroll
    for (int kt = -1; kt < G2_NK - 1; ++kt) {
      WAIT_VM_N(((kt + G2_LA < G2_NK - 1 ? kt + G2_LA : G2_NK - 1) - kt - 1) * 2 * PP);
      RAW_BARRIER();
      if (kt + 1 + G2_LA < G2_NK) stage(kt + 1 + G2_LA);
    }
    return;
  }
  const int g = lane >> 4, q = (lane >> 2) & 3, pp = lane & 3;
  const int wl = wv & 1, wd = wv >> 1;
  const int r15 = lane & 15;
  const size_t oo = (size_t)(l0 + 32 * wl + r15) * D_ + d0 + 32 * wd + 16 * (g & 1) + 8 * (g >> 1);
  u32x4 hres[2];
  hres[0] = *(const u32x4*)((const uint16_t*)(ws + WS_HR) + oo);
  hres[1] = *(const u32x4*)((const uint16_t*)(ws + WS_HR) + oo + 16 * D_);

  int hrd[4];
#pragma unroll
  for (int ks = 0; ks < 4; ++ks) hrd[ks] = (32 * wl + r15) * 256 + (((4 * ks + g) ^ r15) * 16);
  int wrd[2][2];
#pragma unroll
  for (int ci = 0; ci < 2; ++ci)
#pragma unroll
    for (int t = 0; t < 2; ++t) {
      const int row = 8 * g + 4 * t + q;
      const int gf = ((row >> 1) & 1) | (((row >> 3) & 1) << 1);
      wrd[ci][t] = G2_HS + row * 128 + (((2 * (2 * wd + ci)) ^ (2 * gf)) + (pp >> 1)) * 16 + 8 * (pp & 1);
    }
  f32x4 acc[2][2];
  acc[0][0] = acc[0][1] = acc[1][0] = acc[1][1] = f32x4{0, 0, 0, 0};

  struct Frag { bf16x8 h[2], w[2]; };
  auto ldfrag = [&](Frag& F, int kt, int ks) {
    const char* sb = smem + (kt % G2_NS) * G2_STAGE;
    F.h[0] = *(const bf16x8*)(sb + hrd[ks]);
    F.w[0] = cat8(tr_read(sb + wrd[0][0] + ks * 4096), tr_read(sb + wrd[0][1] + ks * 4096));
    F.w[1] = cat8(tr_read(sb + wrd[1][0] + ks * 4096), tr_read(sb + wrd[1][1] + ks * 4096));
    F.h[1] = *(const bf16x8*)(sb + hrd[ks] + 4096);
    __builtin_amdgcn_sched_barrier(0);
  };
  auto mma = [&](const Frag& F) {
#pragma unroll
    for (int a = 0; a < 2; ++a)
#pragma unroll
      for (int ci = 0; ci < 2; ++ci)
        acc[a][ci] = __builtin_amdgcn_mfma_f32_16x16x32_bf16(F.w[ci], F.h[a], acc[a][ci], 0, 0, 0);
    __builtin_amdgcn_sched_barrier(0);
  };
  Frag F0, F1, F2, F3;
  STAMP(1);
  RAW_BARRIER();
  STAMP(2);
  ldfrag(F0, 0, 0);
  ldfrag(F1, 0, 1);
#pragma unroll
  for (int kt = 0; kt < G2_NK; ++kt) {
    if ((kt & 1) == 0) STAMP(3 + kt / 2);
    ldfrag(F2, kt, 2);
    mma(F0);
    ldfrag(F3, kt, 3);
    mma(F1);
    if (kt < G2_NK - 1) {
      if (G2_LA == 4 && kt + 5 < G2_NK) WAIT_LGKM0();
      RAW_BARRIER();
      ldfrag(F0, kt + 1, 0);
    }
    mma(F2);
    if (kt < G2_NK - 1) ldfrag(F1, kt + 1, 1);
    mma(F3);
  }
  STAMP(10);
#pragma unroll
  for (int a = 0; a < 2; ++a) {
    uint32_t a0 = pack2(acc[a][0][0], acc[a][0][1]), a1 = pack2(acc[a][0][2], acc[a][0][3]);
    uint32_t b0 = pack2(acc[a][1][0], acc[a][1][1]), b1 = pack2(acc[a][1][2], acc[a][1][3]);
    swap16(a0, b0); swap16(a1, b1);
    const uint32_t v[4] = {a0, a1, b0, b1};
    u32x4 r;
#pragma unroll
    for (int j = 0; j < 4; ++j) r[j] = pack2(bflo(hres[a][j]) + bflo(v[j]), bfhi(hres[a][j]) + bfhi(v[j]));
    st16<SM4>(out + oo + a * 16 * D_, r);
  }
  STAMP(11);
  STAMP_FLUSH(3);
}

extern "C" void kernel_launch(void* const* d_in, const int* in_sizes, int n_in,
                              void* d_out, int out_size, void* d_ws, size_t ws_size,
                              hipStream_t stream) {
  const uint16_t* x = (const uint16_t*)d_in[0];
  const uint16_t* Are = (const uint16_t*)d_in[1];
  const uint16_t* Aim = (const uint16_t*)d_in[2];
  const uint16_t* Bre = (const uint16_t*)d_in[3];
  const uint16_t* Bim = (const uint16_t*)d_in[4];
  const uint16_t* Cre = (const uint16_t*)d_in[5];
  const uint16_t* Cim = (const uint16_t*)d_in[6];
  const uint16_t* logdt = (const uint16_t*)d_in[7];
  const uint16_t* Dskip = (const uint16_t*)d_in[8];
  const uint16_t* w1 = (const uint16_t*)d_in[10];
  const uint16_t* w2 = (const uint16_t*)d_in[11];
  char* ws = (char*)d_ws;
  (void)hipFuncSetAttribute((const void*)k1_prep, hipFuncAttributeMaxDynamicSharedMemorySize, K1_LDS);
  (void)hipFuncSetAttribute((const void*)k2_conv, hipFuncAttributeMaxDynamicSharedMemorySize, K2_LDS);
  (void)hipFuncSetAttribute((const void*)k3_gemm1, hipFuncAttributeMaxDynamicSharedMemorySize, K3_LDS);
  (void)hipFuncSetAttribute((const void*)k4_gemm2, hipFuncAttributeMaxDynamicSharedMemorySize, K4_LDS);
  k1_prep<<<256, 576, K1_LDS, stream>>>(x, Are, Aim, Bre, Bim, Cre, Cim, logdt, Dskip, ws, (const char*)d_out, (const char*)w1, (const char*)w2);
  k2_conv<<<D_ / 2, 768, K2_LDS, stream>>>(ws);
  k3_gemm1<<<256, 640, K3_LDS, stream>>>(ws);
  k4_gemm2<<<256, 256 + 64 * NLD, K4_LDS, stream>>>(ws);
}
```

```cpp
#include <hip/hip_runtime.h>
#include <hip/hip_bf16.h>
#include <stdint.h>
#include <type_traits>

constexpr int L_ = 2048, D_ = 512, N_ = 128, F_ = 1792;

typedef __attribute__((ext_vector_type(8))) short bf16x8;
typedef __attribute__((ext_vector_type(4))) short bf16x4;
typedef __attribute__((ext_vector_type(2))) short bf16x2_t;
typedef __attribute__((ext_vector_type(8))) _Float16 f16x8;
typedef __attribute__((ext_vector_type(2))) _Float16 f16x2;
typedef __attribute__((ext_vector_type(4))) float f32x4;
typedef __attribute__((ext_vector_type(2))) float f32x2;
typedef __attribute__((ext_vector_type(4))) unsigned u32x4;
typedef __attribute__((ext_vector_type(2))) unsigned u32x2;
typedef __attribute__((address_space(3))) bf16x4 lds_bf16x4;
typedef __attribute__((address_space(3))) char lds_char;

__device__ __forceinline__ float bf2f(uint16_t v) { return __uint_as_float(((uint32_t)v) << 16); }
__device__ __forceinline__ float bflo(uint32_t v) { return __uint_as_float(v << 16); }
__device__ __forceinline__ float bfhi(uint32_t v) { return __uint_as_float(v & 0xffff0000u); }
__device__ __forceinline__ uint16_t f2bf(float f) {
  __hip_bfloat16 b = __float2bfloat16(f);
  return *reinterpret_cast<uint16_t*>(&b);
}
__device__ __forceinline__ float rbf(float f) { return bf2f(f2bf(f)); }
__device__ __forceinline__ uint32_t pack2(float lo, float hi) { return (uint32_t)f2bf(lo) | ((uint32_t)f2bf(hi) << 16); }
__device__ __forceinline__ uint32_t pack2h(float lo, float hi) {
  f16x2 v; v[0] = (_Float16)lo; v[1] = (_Float16)hi;
  return __builtin_bit_cast(uint32_t, v);
}

__device__ __forceinline__ void glds16(const void* g, unsigned lds_dst) {
  unsigned keep;
  asm volatile("s_mov_b32 %0, m0\n\ts_mov_b32 m0, %2\n\ts_nop 0\n\tglobal_load_lds_dwordx4 %1, off\n\ts_mov_b32 m0, %0"
               : "=&s"(keep) : "v"(g), "s"(lds_dst) : "memory");
}
template <int M> __device__ __forceinline__ void st16(void* p, u32x4 v) {
  if (M == 0) *(u32x4*)p = v;
  else if (M == 1) asm volatile("global_store_dwordx4 %0, %1, off nt\n\ts_nop 1" :: "v"(p), "v"(v) : "memory");
  else if (M == 2) asm volatile("global_store_dwordx4 %0, %1, off sc1\n\ts_nop 1" :: "v"(p), "v"(v) : "memory");
  else if (M == 3) asm volatile("global_store_dwordx4 %0, %1, off sc0 sc1\n\ts_nop 1" :: "v"(p), "v"(v) : "memory");
  else asm volatile("global_store_dwordx4 %0, %1, off sc1 nt\n\ts_nop 1" :: "v"(p), "v"(v) : "memory");
}
#define SM1 0
#define SM1K 0
#define GSTAG 12
#define GBAR 2
#define GST0 0
#define SM4 0
#define SM3 0
#define WAIT_VM(n) asm volatile("s_waitcnt vmcnt(" #n ")" ::: "memory")
#define WAIT_LGKM0() asm volatile("s_waitcnt lgkmcnt(0)" ::: "memory")
#define RAW_BARRIER() do { asm volatile("" ::: "memory"); __builtin_amdgcn_s_barrier(); asm volatile("" ::: "memory"); } while (0)

#define STAMP_DECL
#define STAMP(i)
#define STAMP_FLUSH(k)
constexpr size_t WS_UR = 0;
constexpr size_t WS_XT = WS_UR + (size_t)D_ * L_ * 2;
constexpr size_t WS_HT = WS_XT + (size_t)D_ * L_ * 2;
constexpr size_t WS_KG = WS_HT + (size_t)D_ * L_ * 2;
constexpr size_t WS_HR = WS_KG + (size_t)D_ * L_ * 2;
constexpr size_t WS_KS = WS_HR + (size_t)D_ * L_ * 2;
constexpr size_t WS_CS = WS_KS + 4096;
constexpr size_t WS_H1 = WS_CS + 8192;
constexpr size_t WS_ST = WS_UR;

#define CSZ 11232, 14672, 9264
constexpr int CSA[3] = {CSZ};
constexpr int CS2 = CSA[0], CS3 = CSA[1], CS4 = CSA[2];
constexpr int K1_DUMP = 0;
constexpr int K1_LDS = K1_DUMP + 1024;

typedef __attribute__((ext_vector_type(2))) __bf16 bf16v2;
__device__ __forceinline__ uint32_t cvt2(f32x2 v) {
  const bf16v2 b = __builtin_convertvector(v, bf16v2);
  return __builtin_bit_cast(uint32_t, b);
}
__device__ __forceinline__ uint32_t cvt2h(f32x2 v) {
  const f16x2 b = __builtin_convertvector(v, f16x2);
  return __builtin_bit_cast(uint32_t, b);
}
__device__ __forceinline__ f32x2 cmul(f32x2 a, f32x2 b) {
  const f32x2 t = f32x2{a.x, a.x} * b;
  return __builtin_elementwise_fma(f32x2{a.y, a.y}, f32x2{-b.y, b.x}, t);
}
__device__ __forceinline__ f32x2 cexp_(float x, float y) {
  const float m = __expf(x);
  const float hi = y * 0.15915493667125702f;
  const float k = rintf(hi);
  float fr = fmaf(y, 0.15915493667125702f, -k);
  fr = fmaf(y, 6.4206382432985265e-09f, fr);
  return f32x2{m * __builtin_amdgcn_cosf(fr), m * __builtin_amdgcn_sinf(fr)};
}
__device__ __forceinline__ int k_rowbase(int a) { return a < 2 ? 8 * a : a < 4 ? 8 + 4 * a : a < 8 ? 16 + 2 * a : 24 + a; }
template <int C> __device__ __forceinline__ float dppf(float v) {
  return __builtin_bit_cast(float, __builtin_amdgcn_update_dpp(0, __builtin_bit_cast(int, v), C, 0xf, 0xf, true));
}
__device__ __forceinline__ float wave_sum(float v) {
  v += dppf<0xB1>(v); v += dppf<0x4E>(v); v += dppf<0x141>(v); v += dppf<0x140>(v);
  { const auto r = __builtin_amdgcn_permlane16_swap(__float_as_uint(v), __float_as_uint(v), false, false);
    v = __uint_as_float(r[0]) + __uint_as_float(r[1]); }
  { const auto r = __builtin_amdgcn_permlane32_swap(__float_as_uint(v), __float_as_uint(v), false, false);
    v = __uint_as_float(r[0]) + __uint_as_float(r[1]); }
  return v;
}
__device__ __forceinline__ float wave_max(float v) {
  v = fmaxf(v, dppf<0xB1>(v)); v = fmaxf(v, dppf<0x4E>(v)); v = fmaxf(v, dppf<0x141>(v)); v = fmaxf(v, dppf<0x140>(v));
  { const auto r = __builtin_amdgcn_permlane16_swap(__float_as_uint(v), __float_as_uint(v), false, false);
    v = fmaxf(__uint_as_float(r[0]), __uint_as_float(r[1])); }
  { const auto r = __builtin_amdgcn_permlane32_swap(__float_as_uint(v), __float_as_uint(v), false, false);
    v = fmaxf(__uint_as_float(r[0]), __uint_as_float(r[1])); }
  return v;
}

struct PtrRec { const void* p[16]; };
__device__ __attribute__((aligned(128))) PtrRec g_rec[8];
typedef const __attribute__((address_space(4))) uint64_t* crec_t;
#define REC_PTR(T, i) ((T)(__attribute__((address_space(1))) char*)((crec_t)(const void*)&g_rec[blockIdx.x & 7])[i])
extern "C" __global__ __launch_bounds__(576) void k1_prep(
    const uint16_t* __restrict__ x, const uint16_t* __restrict__ Are, const uint16_t* __restrict__ Aim,
    const uint16_t* __restrict__ Bre, const uint16_t* __restrict__ Bim, const uint16_t* __restrict__ Cre,
    const uint16_t* __restrict__ Cim, const uint16_t* __restrict__ logdt, const uint16_t* __restrict__ Dskip,
    char* __restrict__ ws, const char* __restrict__ outp, const char* __restrict__ w1p, const char* __restrict__ w2p) {
  extern __shared__ __attribute__((aligned(16))) char smem[];
  const int tid = threadIdx.x, lane = tid & 63;
  const int wv = __builtin_amdgcn_readfirstlane(tid >> 6);
  const int bid = blockIdx.x;
  STAMP_DECL
  const int l0 = bid * 8;
  if (wv == 8) {
    const unsigned dump = (unsigned)(size_t)(lds_char*)smem + K1_DUMP;
    {
      const char* pc;
      int o2, o3, o4;
      asm volatile("s_getpc_b64 %0\n\ts_mov_b32 %1, k2_conv@rel32@lo+4\n\ts_mov_b32 %2, k3_gemm1@rel32@lo+12\n\t"
                   "s_mov_b32 %3, k4_gemm2@rel32@lo+20" : "=s"(pc), "=s"(o2), "=s"(o3), "=s"(o4));
      const int o = (bid >> 3) * 512 + (lane & 31) * 16;
      glds16(pc + (lane < 32 ? o2 + min(o, CS2 - 16) : o4 + min(o, CS4 - 16)), dump);
      glds16(pc + (o3 + min(o, CS3 - 16)), dump);
    }
    if (bid < 8 && lane < 16) {
      const void* p = lane == 0 ? (const void*)Are : lane == 1 ? (const void*)Aim : lane == 2 ? (const void*)Bre
                    : lane == 3 ? (const void*)Bim : lane == 4 ? (const void*)Cre : lane == 5 ? (const void*)Cim
                    : lane == 6 ? (const void*)logdt : lane == 7 ? (const void*)Dskip : lane == 8 ? (const void*)w1p
                    : lane == 9 ? (const void*)w2p : lane == 10 ? (const void*)ws : lane == 11 ? (const void*)outp : (const void*)x;
      g_rec[bid].p[lane] = p;
    }
    const int dbase = 64 * (bid & 7) + 2 * (bid >> 3);
    const int po = dbase * N_ + (lane & 31) * 8;
    glds16(x + (size_t)((bid >> 3) * 64 + lane) * D_ + (bid & 7) * 64, dump);
    glds16((lane < 32 ? Are : Aim) + po, dump);
    glds16((lane < 32 ? Bre : Bim) + po, dump);
    glds16((lane < 32 ? Cre : Cim) + po, dump);
    glds16((lane < 32 ? logdt : Dskip) + (dbase & ~7), dump);
    return;
  }
  const u32x4 xr = *(const u32x4*)(x + (size_t)(l0 + wv) * D_ + lane * 8);
  STAMP(1);
  {
    f32x2 v[4];
#pragma unroll
    for (int j = 0; j < 4; ++j) v[j] = f32x2{bflo(xr[j]), bfhi(xr[j])};
    const f32x2 s2 = (v[0] + v[1]) + (v[2] + v[3]);
    const float mean = wave_sum(s2.x + s2.y) * (1.0f / D_);
    f32x2 q2 = f32x2{0.f, 0.f};
#pragma unroll
    for (int j = 0; j < 4; ++j) { const f32x2 dd = v[j] - mean; q2 = __builtin_elementwise_fma(dd, dd, q2); }
    const float var = wave_sum(q2.x + q2.y) * (1.0f / D_);
    const float m = rbf(mean);
    const float sd = rbf(__builtin_amdgcn_sqrtf(rbf(rbf(var) + 1e-5f)));
    if (lane == 0) *(f32x2*)(ws + WS_ST + (size_t)(l0 + wv) * 8) = f32x2{m, 1.0f / sd};
  }
  STAMP(3);
  STAMP_FLUSH(0);
}

constexpr int UC_S = 4160;
constexpr int UC_B4 = 4 * UC_S + 32;
constexpr int UREG = UC_B4 + 4 * UC_S;
constexpr int KOFF = UREG;
constexpr int T_OFF = KOFF + 512 + 4096;
constexpr int T_RS = 528;
constexpr int KF_OFF = T_OFF + 57 * T_RS;
constexpr int XL_OFF = KF_OFF + 3104;
constexpr int CH_LDS = XL_OFF + 4096;
constexpr int UL_OFF = 3 * UC_S + UC_B4;
constexpr int K2_CNT = 2 * CH_LDS + 1024;
constexpr int K2_LDS = K2_CNT + 64;

extern "C" __global__ __launch_bounds__(768) void k2_conv(char* __restrict__ ws_) {
  const uint16_t* __restrict__ Are = REC_PTR(const uint16_t*, 0);
  const uint16_t* __restrict__ Aim = REC_PTR(const uint16_t*, 1);
  const uint16_t* __restrict__ Bre = REC_PTR(const uint16_t*, 2);
  const uint16_t* __restrict__ Bim = REC_PTR(const uint16_t*, 3);
  const uint16_t* __restrict__ Cre = REC_PTR(const uint16_t*, 4);
  const uint16_t* __restrict__ Cim = REC_PTR(const uint16_t*, 5);
  const uint16_t* __restrict__ logdt = REC_PTR(const uint16_t*, 6);
  const uint16_t* __restrict__ Dskip = REC_PTR(const uint16_t*, 7);
  const uint16_t* __restrict__ w1 = REC_PTR(const uint16_t*, 8);
  const uint16_t* __restrict__ w2 = REC_PTR(const uint16_t*, 9);
  char* __restrict__ ws = REC_PTR(char*, 10);
  const uint16_t* __restrict__ x = REC_PTR(const uint16_t*, 12);
  extern __shared__ __attribute__((aligned(16))) char smem[];
  const int tid = threadIdx.x, lane = tid & 63;
  const int wv = __builtin_amdgcn_readfirstlane(tid >> 6);
  const int bid = blockIdx.x;
  STAMP_DECL
  const int m = lane & 15, kg = lane >> 4;
  const int dbase = 64 * (bid & 7) + 2 * (bid >> 3);
  const int chs = (wv >> 1) & 1, half = wv & 1;
  typedef __attribute__((address_space(3))) uint32_t lds_u32;
  if (wv >= 8) {
    const int i = tid - 512, w = wv - 8;
    uint32_t xv[8], xp;
    f32x4 sv[4];
    f32x2 sp;
    STAMP(1);
#define LDS4(j) asm volatile("global_load_dwordx4 %0, %1, %2 offset:" #j "*16" : "=v"(sv[j]) : "v"(i * 64u), "s"(ws + WS_ST))
    LDS4(0); LDS4(1); LDS4(2); LDS4(3);
    const unsigned rp = max(512 * w - 8 + min(lane, 7), 0);
    asm volatile("global_load_dwordx2 %0, %1, %2" : "=v"(sp) : "v"(rp * 8u), "s"(ws + WS_ST));
    if (w >= GBAR) RAW_BARRIER();
    for (int k = 0; k < (w >= GBAR ? w - GBAR : w); ++k) __builtin_amdgcn_s_sleep(GSTAG);
    if (w >= GBAR) __builtin_amdgcn_s_sleep(GST0);
    const unsigned xo = i * 8192u + dbase * 2u;
    asm volatile("global_load_dword %0, %1, %2" : "=v"(xp) : "v"(rp * 1024u + dbase * 2u), "s"(x));
#define LDX(j, o) asm volatile("global_load_dword %0, %1, %2 offset:" #o : "=v"(xv[j]) : "v"(xo + (j & 4) * 1024u), "s"(x))
    LDX(0, 0); LDX(1, 1024); LDX(2, 2048); LDX(3, 3072); LDX(4, 0); LDX(5, 1024); LDX(6, 2048); LDX(7, 3072);
    if (w < GBAR) RAW_BARRIER();
    STAMP(2);
    asm volatile("s_waitcnt vmcnt(8)" : "+v"(xp), "+v"(sp), "+v"(sv[0]), "+v"(sv[1]), "+v"(sv[2]), "+v"(sv[3]));
    auto pkh = [](float lo, float hi) { return __builtin_bit_cast(uint32_t, __builtin_amdgcn_cvt_pkrtz(lo, hi)); };
    uint32_t P0[4], P1[4];
    {
      const uint32_t a = cvt2(f32x2{bflo(xp), bfhi(xp)} - sp[0]);
      const uint32_t u = cvt2(f32x2{bflo(a), bfhi(a)} * sp[1]);
      const uint32_t h = w ? pkh(bflo(u), bfhi(u)) : 0u;
#pragma unroll
      for (int j = 0; j < 4; ++j) {
        const uint32_t e1 = __builtin_amdgcn_readlane(h, 7 - 2 * j), e0 = __builtin_amdgcn_readlane(h, 6 - 2 * j);
        P0[j] = (e1 & 0xffffu) | (e0 << 16);
        P1[j] = (e1 >> 16) | (e0 & 0xffff0000u);
      }
    }
    uint32_t S0[8], S1[8];
    u32x4 x0, x1;
    auto pair = [&](auto J) {
      constexpr int j = decltype(J)::value;
      asm volatile("s_waitcnt vmcnt(%2)" : "+v"(xv[2 * j]), "+v"(xv[2 * j + 1]) : "i"(6 - 2 * j));
      uint32_t ub[2];
#pragma unroll
      for (int k = 0; k < 2; ++k) {
        const uint32_t a = cvt2(f32x2{bflo(xv[2 * j + k]), bfhi(xv[2 * j + k])} - sv[j][2 * k]);
        ub[k] = cvt2(f32x2{bflo(a), bfhi(a)} * sv[j][2 * k + 1]);
      }
      S0[3 - j] = pkh(bflo(ub[1]), bflo(ub[0]));
      S1[3 - j] = pkh(bfhi(ub[1]), bfhi(ub[0]));
      x0[j] = __builtin_amdgcn_perm(xv[2 * j + 1], xv[2 * j], 0x05040100u);
      x1[j] = __builtin_amdgcn_perm(xv[2 * j + 1], xv[2 * j], 0x07060302u);
      S0[7 - j] = __builtin_amdgcn_update_dpp(P0[3 - j], S0[3 - j], 0x138, 0xf, 0xf, false);
      S1[7 - j] = __builtin_amdgcn_update_dpp(P1[3 - j], S1[3 - j], 0x138, 0xf, 0xf, false);
      asm volatile("" : "+v"(S0[7 - j]), "+v"(S1[7 - j]), "+v"(x0[j]), "+v"(x1[j]));
    };
    pair(std::integral_constant<int, 0>{});
    pair(std::integral_constant<int, 1>{});
    pair(std::integral_constant<int, 2>{});
    pair(std::integral_constant<int, 3>{});
    STAMP(3);
    char* uc = smem + 4080 - 16 * i;
#pragma unroll
    for (int ml = 7; ml >= 0; --ml) {
      const int s = 7 - ml;
      u32x4 o0, o1;
#pragma unroll
      for (int k = 0; k < 4; ++k) {
        o0[k] = (s & 1) ? __builtin_amdgcn_alignbit(S0[(s >> 1) + k + 1], S0[(s >> 1) + k], 16) : S0[(s >> 1) + k];
        o1[k] = (s & 1) ? __builtin_amdgcn_alignbit(S1[(s >> 1) + k + 1], S1[(s >> 1) + k], 16) : S1[(s >> 1) + k];
      }
      *(u32x4*)(uc + (ml & 3) * UC_S + (ml >> 2) * UC_B4) = o0;
      *(u32x4*)(uc + CH_LDS + (ml & 3) * UC_S + (ml >> 2) * UC_B4) = o1;
    }
    *(u32x4*)(smem + XL_OFF + 16 * i) = x0;
    *(u32x4*)(smem + CH_LDS + XL_OFF + 16 * i) = x1;
    WAIT_LGKM0();
    STAMP(4);
    RAW_BARRIER();
    {
      const char* src = (const char*)w1 + (size_t)(bid >> 3) * 16 * (F_ * 2) + (bid & 1) * F_;
      const unsigned dump = (unsigned)(size_t)(lds_char*)smem + 2 * CH_LDS;
#pragma unroll
      for (int j = 0; j < 7; ++j) {
        const unsigned c = (w * 7 + j) * 64 + lane, r = c / 112u;
        glds16(src + r * (F_ * 2) + (c - r * 112u) * 16u, dump);
      }
#pragma unroll
      for (int j = 0; j < 2; ++j)
        glds16((const char*)w2 + (size_t)bid * 7168 + min(w * 2 + j, 6) * 1024 + lane * 16, dump);
    }
    WAIT_VM(0);
    return;
  }
  uint32_t pds;
  int sexp;
  {
    const int ch = wv >> 2, wq = wv & 3, part = wq >> 1;
    const int t8 = tid & 255, n = tid & 127;
    const int d = dbase + ch;
    char* cb = smem + ch * CH_LDS;
    char* tb = cb + T_OFF;
    float* Kf = (float*)(cb + KF_OFF);
    uint32_t pAr, pAi, pBr, pBi, pCr, pCi, pdt;
    {
      const unsigned po = (d * N_ + n) * 2u;
#define LD16(r, o, p) asm volatile("global_load_ushort %0, %1, %2" : "=v"(r) : "v"(o), "s"(p))
      LD16(pdt, d * 2u, logdt); LD16(pAr, po, Are); LD16(pAi, po, Aim); LD16(pBr, po, Bre); LD16(pBi, po, Bim);
      LD16(pCr, po, Cre); LD16(pCi, po, Cim); LD16(pds, (dbase + chs) * 2u, Dskip);
    }
    lds_u32* cnt = (lds_u32*)(smem + K2_CNT) + 4 * ch;
    auto gsync = [&](int ph) {
      asm volatile("" ::: "memory");
      if (lane == 0) __hip_atomic_fetch_add(cnt + ph, 1u, __ATOMIC_RELAXED, __HIP_MEMORY_SCOPE_WORKGROUP);
      while (__builtin_amdgcn_readfirstlane(*(volatile lds_u32*)(cnt + ph)) < 4u) __builtin_amdgcn_s_sleep(1);
      asm volatile("" ::: "memory");
    };
    if (t8 >= 64 && t8 < 68) cnt[t8 - 64] = 0u;
    if (t8 < 32) *(u32x4*)(cb + KOFF + t8 * 16) = u32x4{0, 0, 0, 0};
    else if (t8 < 40) {
      const int j = t8 - 32;
      *(u32x4*)(cb + (j & 3) * UC_S + (j >> 2) * UC_B4 + 256 * 16) = u32x4{0, 0, 0, 0};
    }
    asm volatile("s_waitcnt vmcnt(0)" : "+v"(pdt), "+v"(pAr), "+v"(pAi), "+v"(pBr), "+v"(pBi), "+v"(pCr), "+v"(pCi), "+v"(pds));
    const float dt = rbf(expf(bflo(pdt)));
    const float zx = dt * bflo(pAr);
    const float zy = dt * bflo(pAi);
    f32x2 c;
    {
      const float br = dt * bflo(pBr), bi = dt * bflo(pBi);
      const float cr = bflo(pCr), ci = bflo(pCi);
      c = f32x2{cr * br - ci * bi, -(cr * bi + ci * br)};
    }
    {
      const float mg = wave_max(fmaxf(fabsf(c.x), fabsf(c.y)) * fmaxf(1.f, __expf(2048.f * zx)));
      if (lane == 0 && part == 0) Kf[772 + (wq & 1)] = mg;
    }
    auto putn = [&](auto NV, const int* rows, const f32x2* v) {
      constexpr int nv = decltype(NV)::value;
      uint32_t H[nv];
#pragma unroll
      for (int i = 0; i < nv; ++i) H[i] = cvt2h(v[i]);
#pragma unroll
      for (int i = 0; i < nv; ++i) *(uint32_t*)(tb + rows[i] * T_RS + 4 * n) = H[i];
    };
    auto cmuln = [&](auto NV, f32x2* o, const f32x2* v, f32x2 mm) {
      constexpr int nv = decltype(NV)::value;
      const f32x2 mn = f32x2{-mm.y, mm.x};
      f32x2 t[nv];
#pragma unroll
      for (int i = 0; i < nv; ++i) t[i] = f32x2{v[i].x, v[i].x} * mm;
#pragma unroll
      for (int i = 0; i < nv; ++i) o[i] = __builtin_elementwise_fma(f32x2{v[i].y, v[i].y}, mn, t[i]);
    };
    using I4 = std::integral_constant<int, 4>;
    using I8 = std::integral_constant<int, 8>;
    using I6 = std::integral_constant<int, 6>;
    using I3 = std::integral_constant<int, 3>;
    STAMP(1);
    const f32x2 g1 = cexp_(128.f * zx, -128.f * zy);
    const f32x2 ea = cexp_(part ? 2.f * zx : zx, part ? -2.f * zy : -zy);
    WAIT_LGKM0();
    RAW_BARRIER();
    {
      const f32x2 m2 = *(const f32x2*)(Kf + 772);
      const int ex = (int)((__float_as_uint(fmaxf(m2.x, m2.y)) >> 23) & 255u) - 127;
      sexp = 13 - ex;
      sexp = sexp > 126 ? 126 : (sexp < -126 ? -126 : sexp);
      c *= __uint_as_float((uint32_t)(127 + sexp) << 23);
    }
    STAMP(2);
    if (part == 0) {
      const f32x2 e1 = ea;
      const f32x2 e8 = cexp_(8.f * zx, 8.f * zy);
      const f32x2 e2 = cmul(e1, e1), e16 = cmul(e8, e8);
      const f32x2 e4 = cmul(e2, e2), e32 = cmul(e16, e16);
      f32x2 A[8], Bv[4];
      A[0] = c; A[1] = cmul(c, e1); A[2] = cmul(c, e2);
      Bv[0] = f32x2{1.f, 0.f}; Bv[1] = e8; Bv[2] = e16; Bv[3] = cmul(e16, e8);
      A[3] = cmul(A[1], e2);
      cmuln(I4{}, A + 4, A, e4);
      { const int r[4] = {41, 42, 43, 44}; putn(I4{}, r, Bv); }
      cmuln(I4{}, Bv, Bv, e32);
      { const int r[8] = {0, 1, 2, 3, 4, 5, 6, 7}; putn(I8{}, r, A); }
      cmuln(I8{}, A, A, g1);
      { const int r[4] = {45, 46, 47, 48}; putn(I4{}, r, Bv); }
      cmuln(I4{}, Bv, Bv, e32);
      { const int r[8] = {8, 9, 10, 11, 12, 13, 14, 15}; putn(I8{}, r, A); }
      { const int r[4] = {49, 50, 51, 52}; putn(I4{}, r, Bv); }
      cmuln(I4{}, Bv, Bv, e32);
      { const int r[4] = {53, 54, 55, 56}; putn(I4{}, r, Bv); }
    } else {
      const f32x2 e2 = ea;
      const f32x2 g2 = cmul(g1, g1), e4 = cmul(e2, e2);
      const f32x2 g4 = cmul(g2, g2);
      f32x2 Q[8], R[6], S[4];
      Q[0] = cmul(c, g2);
      Q[1] = cmul(Q[0], g1); Q[2] = cmul(Q[0], g2);
      Q[3] = cmul(Q[2], g1);
      cmuln(I4{}, Q + 4, Q, g4);
      cmuln(I6{}, R, Q, e4);
      { f32x2 t[4] = {Q[0], R[0], Q[1], R[1]}; cmuln(I4{}, S, t, e2); }
      { const int r[8] = {16, 20, 24, 26, 28, 30, 32, 33}; putn(I8{}, r, Q); }
      cmuln(I4{}, Q, Q + 4, g4);
      { const int r[6] = {18, 22, 25, 27, 29, 31}; putn(I6{}, r, R); }
      cmuln(I3{}, Q + 4, Q, g4);
      { const int r[4] = {17, 19, 21, 23}; putn(I4{}, r, S); }
      { const int r[4] = {34, 35, 36, 37}; putn(I4{}, r, Q); }
      { const int r[3] = {38, 39, 40}; putn(I3{}, r, Q + 4); }
    }
    STAMP(3);
    gsync(1);
    STAMP(4);
    if (wq < 3) {
      const char* ta = tb + (16 * wq + m) * T_RS + kg * 16;
      const char* tq = tb + (41 + m) * T_RS + kg * 16;
      f32x4 a1 = f32x4{0, 0, 0, 0}, a2 = f32x4{0, 0, 0, 0};
      f16x8 Ah[8], Bh[8];
#pragma unroll
      for (int s = 0; s < 8; ++s) { Ah[s] = *(const f16x8*)(ta + 64 * s); Bh[s] = *(const f16x8*)(tq + 64 * s); }
      __builtin_amdgcn_sched_barrier(0);
#pragma unroll
      for (int s = 0; s < 8; s += 2) {
        a1 = __builtin_amdgcn_mfma_f32_16x16x32_f16(Ah[s], Bh[s], a1, 0, 0, 0);
        a2 = __builtin_amdgcn_mfma_f32_16x16x32_f16(Ah[s + 1], Bh[s + 1], a2, 0, 0, 0);
      }
      a1 += a2;
      float mx = 0.f;
#pragma unroll
      for (int j = 0; j < 4; ++j) {
        const int ix = (16 * wq + 4 * kg + j) * 16 + m;
        Kf[ix] = a1[j];
        if (ix <= 640) mx = fmaxf(mx, fabsf(a1[j]));
      }
      mx = wave_max(mx);
      if (lane == 0) Kf[768 + wq] = mx;
    }
    STAMP(5);
    gsync(2);
    STAMP(6);
    {
      const int a = t8 >> 4, b = t8 & 15;
      const float* kp = Kf + k_rowbase(a) * 16 + b;
      float kv[8];
#pragma unroll
      for (int i = 0; i < 8; ++i) kv[i] = kp[16 * i];
      float vn = Kf[k_rowbase((t8 + 1) >> 4) * 16 + ((t8 + 1) & 15)];
      const f32x4 m4 = *(const f32x4*)(Kf + 768);
      __builtin_amdgcn_sched_barrier(0);
      const float mx = fmaxf(fmaxf(m4[0], m4[1]), m4[2]);
      const int ex = (int)((__float_as_uint(mx) >> 23) & 255u) - 127;
      int sh = 14 - ex;
      sh = sh > 126 ? 126 : (sh < -126 ? -126 : sh);
      const float scale = __uint_as_float((uint32_t)(127 + sh) << 23);
      if (t8 == 0) {
        int e = -(sh + sexp);
        e = e > 126 ? 126 : (e < -126 ? -126 : e);
        Kf[774] = __uint_as_float((uint32_t)(127 + e) << 23);
      }
      float v[8];
      {
        const bool c2 = a < 2, c4 = a < 4, c8 = a < 8;
        const float w0 = kv[0], w1_ = kv[1], w2_ = kv[2], w3 = kv[3];
        v[0] = w0;
        v[1] = c2 ? w1_ : w0;
        v[2] = c2 ? w2_ : c4 ? w1_ : w0;
        v[3] = c2 ? w3 : c4 ? w2_ : c8 ? w1_ : w0;
        v[4] = c2 ? kv[4] : c4 ? w2_ : c8 ? w1_ : ((t8 & 1) ? vn : w0);
        v[5] = c2 ? kv[5] : c4 ? w2_ : c8 ? w1_ : vn;
        v[6] = c2 ? kv[6] : c4 ? w3 : vn;
        v[7] = c2 ? kv[7] : vn;
#pragma unroll
        for (int i = 0; i < 8; ++i) v[i] *= scale;
      }
      const u32x4 kk = u32x4{pack2h(v[0], v[1]), pack2h(v[2], v[3]), pack2h(v[4], v[5]), pack2h(v[6], v[7])};
      *(u32x4*)(cb + KOFF + 512 + t8 * 16) = kk;
    }
    __builtin_amdgcn_sched_barrier(0);
    STAMP(7);
  }
  __syncthreads();
  STAMP(8);
  if (wv >= 4) return;
  u32x2 xe[4], ue[4];
#pragma unroll
  for (int j = 0; j < 4; ++j) {
    const int t0 = 256 * (4 * half + j) + 16 * m + 4 * kg;
    xe[j] = *(const u32x2*)(smem + chs * CH_LDS + XL_OFF + 2 * t0);
    ue[j] = *(const u32x2*)(smem + chs * CH_LDS + UL_OFF + 2 * (2044 - t0));
  }

  char* cb = smem + chs * CH_LDS;
  const int ml = m & 7, mh = m >> 3;
  const char* abase = cb + (ml & 3) * UC_S + (ml >> 2) * UC_B4 + 16 * (253 - mh + kg) - 256 * half;
  const char* bbase = cb + KOFF + 512 + 32 * m + 16 * kg - 32 - 256 * half;
  f32x4 acc[8];
#pragma unroll
  for (int t = 0; t < 8; ++t) acc[t] = f32x4{0, 0, 0, 0};
  f16x8 af[2][8], bfr[2][8];
  af[0][0] = *(const f16x8*)(abase);
#pragma unroll
  for (int j = 0; j < 8; ++j) bfr[0][j] = *(const f16x8*)(bbase + 512 * j);
#pragma unroll
  for (int j = 1; j < 8; ++j) af[0][j] = *(const f16x8*)(abase - 512 * j);
  const float kinv = ((const float*)(cb + KF_OFF))[774];
  uint32_t ud[4][2];
  {
    const float dsk = bflo(pds);
#pragma unroll
    for (int j = 0; j < 4; ++j)
#pragma unroll
      for (int r2 = 0; r2 < 2; ++r2) {
        const uint32_t uw = ue[j][1 - r2];
        const f16x2 u2 = __builtin_bit_cast(f16x2, uw);
        ud[j][r2] = cvt2(f32x2{(float)u2[1], (float)u2[0]} * dsk);
        asm volatile("" : "+v"(ud[j][r2]));
      }
  }
  __builtin_amdgcn_sched_barrier(0);
#pragma unroll
  for (int s = 0; s < 4; ++s) {
    int slot = 0;
#pragma unroll
    for (int al = 0; al < 8; ++al)
#pragma unroll
      for (int T = al; T < 8; ++T) {
        acc[T] = __builtin_amdgcn_mfma_f32_16x16x32_f16(af[s & 1][al], bfr[s & 1][T - al], acc[T], 0, 0, 0);
        __builtin_amdgcn_sched_barrier(0);
        if (s < 3 && (slot & 1) == 0 && slot < 32) {
          const int j = slot >> 1;
          if (j >= 1 && j <= 8) bfr[(s + 1) & 1][j - 1] = *(const f16x8*)(bbase + 512 * (j - 1) - 64 * (s + 1));
          else { const int ja = j == 0 ? 0 : j - 8; af[(s + 1) & 1][ja] = *(const f16x8*)(abase - 512 * ja - 64 * (s + 1)); }
          __builtin_amdgcn_sched_barrier(0);
        }
        ++slot;
      }
  }
  STAMP(9);
  char* xb = cb + T_OFF;
  f32x4 keep[4];
  if (half) {
#pragma unroll
    for (int j = 0; j < 4; ++j) { *(f32x4*)(xb + 4096 + j * 1024 + lane * 16) = acc[j]; keep[j] = acc[4 + j]; }
    asm volatile("" ::: "memory");
  } else {
#pragma unroll
    for (int j = 0; j < 4; ++j) { *(f32x4*)(xb + j * 1024 + lane * 16) = acc[4 + j]; keep[j] = acc[j]; }
    asm volatile("" ::: "memory");
  }
  {
    lds_u32* cn = (lds_u32*)(smem + K2_CNT) + 4 * chs + 3;
    if (lane == 0) __hip_atomic_fetch_add(cn, 1u, __ATOMIC_RELAXED, __HIP_MEMORY_SCOPE_WORKGROUP);
    while (__builtin_amdgcn_readfirstlane(*(volatile lds_u32*)cn) < 2u) __builtin_amdgcn_s_sleep(0);
    asm volatile("" ::: "memory");
  }
  STAMP(10);
  uint16_t* hT = (uint16_t*)(ws + WS_HT) + (size_t)(dbase + chs) * L_;
#pragma unroll
  for (int j = 0; j < 4; ++j) {
    const f32x4 o = *(const f32x4*)(xb + (1 - half) * 4096 + j * 1024 + lane * 16);
    const f32x4 a = keep[j];
    u32x2 hv;
#pragma unroll
    for (int r2 = 0; r2 < 2; ++r2) {
      const uint32_t y = cvt2((f32x2{a[2 * r2], a[2 * r2 + 1]} + f32x2{o[2 * r2], o[2 * r2 + 1]}) * kinv);
      const uint32_t t = cvt2(f32x2{bflo(y), bfhi(y)} + f32x2{bflo(ud[j][r2]), bfhi(ud[j][r2])});
      hv[r2] = cvt2(f32x2{bflo(t), bfhi(t)} + f32x2{bflo(xe[j][r2]), bfhi(xe[j][r2])});
    }
    *(u32x2*)(hT + 256 * (4 * half + j) + 16 * m + 4 * kg) = hv;
  }
  STAMP(11);
  STAMP_FLUSH(1);
}

constexpr int G1_HS = 64 * 256;
constexpr int G1_WS = 64 * 224;
constexpr int G1_STAGE = G1_HS + G1_WS;
constexpr int G1_NS = 5;
constexpr int G1_ST = G1_NS * G1_STAGE;
constexpr int G1_DUMP = G1_ST + 2048;
constexpr int K3_LDS = G1_DUMP + 1024;
#define G1_LA 2
#define TPRIO 1
#define TPRE 2
#define PF2_A 0
#define PF2_C 10
#define PF2_D 6
__device__ constexpr int g1_pfs(int kt) {
  return kt <= -1 ? 0 : kt <= 5 ? PF2_A * (kt + 1) : kt == 6 ? PF2_A * 6 + PF2_C : 28 - PF2_D;
}

__device__ __forceinline__ bf16x4 tr_read(const char* p) {
  return __builtin_amdgcn_ds_read_tr16_b64_v4i16((lds_bf16x4*)p);
}
__device__ __forceinline__ bf16x8 cat8(bf16x4 a, bf16x4 b) {
  return bf16x8{a[0], a[1], a[2], a[3], b[0], b[1], b[2], b[3]};
}
__device__ __forceinline__ void swap16(uint32_t& a, uint32_t& b) {
  auto r = __builtin_amdgcn_permlane16_swap(a, b, false, false);
  a = r[0]; b = r[1];
}
__device__ __forceinline__ float sum_g(float t) {
  { const auto r = __builtin_amdgcn_permlane16_swap(__float_as_uint(t), __float_as_uint(t), false, false);
    t = __uint_as_float(r[0]) + __uint_as_float(r[1]); }
  { const auto r = __builtin_amdgcn_permlane32_swap(__float_as_uint(t), __float_as_uint(t), false, false);
    t = __uint_as_float(r[0]) + __uint_as_float(r[1]); }
  return t;
}
#define WAIT_VM_N(n) asm volatile("s_waitcnt vmcnt(%0)" :: "i"(n) : "memory")
__device__ __forceinline__ void glds16s(unsigned voff, const void* sbase, unsigned lds_dst) {
  asm volatile("s_mov_b32 m0, %2\n\ts_nop 0\n\tglobal_load_lds_dwordx4 %0, %1" :: "v"(voff), "s"(sbase), "s"(lds_dst)
               : "memory");
}

template <int WF>
__device__ __forceinline__ void k3_body(char* __restrict__ ws, char* smem,
                                        const int lane, const int wv, const int l0, const int f0, const int ft) {
  constexpr int NT = WF ? 3 : 4;
  constexpr int CI0 = WF ? 4 : 0;
  STAMP_DECL
  const int wl = wv & 3;
  const unsigned lbase = (unsigned)(size_t)(lds_char*)smem;
  const int g = lane >> 4, q = (lane >> 2) & 3, pp = lane & 3, r15 = lane & 15;
  int tix[NT];
#pragma unroll
  for (int i = 0; i < NT; ++i) {
    int t = i + (WF ? (wl == 3 ? 0 : wl) : wl);
    t = t >= NT ? t - NT : t;
    tix[i] = CI0 + t;
  }
  const int xv = 2 * (4 * (g & 1) + q);
  unsigned hb[3][2], wb[3][NT];
#pragma unroll
  for (int r = 0; r < 3; ++r) {
#pragma unroll
    for (int cc = 0; cc < 2; ++cc) {
      hb[r][cc] = lbase + r * 2 * G1_STAGE + (4 * g + q) * 256 + ((((2 * (2 * wl + cc)) ^ xv) + (pp >> 1)) * 16) + 8 * (pp & 1);
      asm volatile("" : "+v"(hb[r][cc]));
    }
#pragma unroll
    for (int i = 0; i < NT; ++i) {
      wb[r][i] = lbase + r * 2 * G1_STAGE + G1_HS + (4 * g + q) * 224 + 8 * pp + 32 * tix[i];
      asm volatile("" : "+v"(wb[r][i]));
    }
  }
  const int lrow = l0 + 32 * wl + r15;

  f32x4 acc[NT][2];
#pragma unroll
  for (int i = 0; i < NT; ++i) { acc[i][0] = f32x4{0, 0, 0, 0}; acc[i][1] = f32x4{0, 0, 0, 0}; }
  f32x4 accS[2], accG[2], accC;
  accS[0] = accS[1] = accG[0] = accG[1] = accC = f32x4{0, 0, 0, 0};
  bf16x8 ones = {0x3f80, 0x3f80, 0x3f80, 0x3f80, 0x3f80, 0x3f80, 0x3f80, 0x3f80};
  asm volatile("" : "+v"(ones));

  struct Frag { bf16x8 h[2], w[NT]; };
  auto trl = [&](unsigned a) { return __builtin_amdgcn_ds_read_tr16_b64_v4i16((lds_bf16x4*)(size_t)a); };
  auto ld1 = [&](Frag& F, int kt, int ks, int i) {
    const int bf = kt % G1_NS, r = bf >> 1, o = (bf & 1) * G1_STAGE;
    if (i < 2) F.h[i] = cat8(trl(hb[r][i] + o + ks * 8192), trl(hb[r][i] + o + ks * 8192 + 4096));
    else F.w[i - 2] = cat8(trl(wb[r][i - 2] + o + ks * 32 * 224), trl(wb[r][i - 2] + o + (ks * 32 + 16) * 224));
  };
  constexpr int NM = 2 * NT + 1 + (WF ? 4 : 0);
  auto mm1 = [&](const Frag& F, int i) {
    if (i < 2 * NT) {
      const int ci = i >> 1, cc = i & 1;
      acc[ci][cc] = __builtin_amdgcn_mfma_f32_16x16x32_bf16(F.w[ci], F.h[cc], acc[ci][cc], 0, 0, 0);
    } else if (i == 2 * NT) {
      accC = __builtin_amdgcn_mfma_f32_16x16x32_bf16(F.w[0], ones, accC, 0, 0, 0);
    } else {
      const int cc = i & 1;
      if (i < 2 * NT + 3) accS[cc] = __builtin_amdgcn_mfma_f32_16x16x32_bf16(ones, F.h[cc], accS[cc], 0, 0, 0);
      else accG[cc] = __builtin_amdgcn_mfma_f32_16x16x32_bf16(F.h[cc], F.h[cc], accG[cc], 0, 0, 0);
    }
  };
  auto step = [&](const Frag& C, Frag& N, int nkt, int nks, int kstep) {
#pragma unroll
    for (int i = 0; i < NM; ++i) {
      mm1(C, i);
      __builtin_amdgcn_sched_barrier(0);
      if (nkt >= 0 && i < NT + 2) ld1(N, nkt, nks, i);
      __builtin_amdgcn_sched_barrier(0);
    }
    if (!WF && kstep < 2) {
      if (kstep == (ft & 1)) {
        asm volatile("" ::: "memory");
#pragma unroll
        for (int cc = 0; cc < 2; ++cc) {
          const u32x4 hv = __builtin_bit_cast(u32x4, C.h[cc]);
          uint32_t a0 = hv[0], a1 = hv[1], b0 = hv[2], b1 = hv[3];
          swap16(a0, b0); swap16(a1, b1);
          uint16_t* p = (uint16_t*)(ws + WS_HR) + (size_t)(lrow + 16 * cc) * D_ + 32 * ft + 16 * (g & 1) + 8 * (g >> 1);
          *(u32x4*)p = u32x4{a0, a1, b0, b1};
        }
      }
      __builtin_amdgcn_sched_barrier(0);
    }
  };
  Frag F0, F1;
  STAMP(1);
  RAW_BARRIER();
  STAMP(2);
#pragma unroll
  for (int i = 0; i < NT + 2; ++i) ld1(F0, 0, 0, i);
#pragma unroll
  for (int kt = 0; kt < 7; ++kt) {
    STAMP(3 + kt);
    step(F0, F1, kt, 1, 2 * kt);
    if (G1_LA == G1_NS - 1 && kt + G1_NS < 8) WAIT_LGKM0();
    RAW_BARRIER();
    if (kt < 6) step(F1, F0, kt + 1, 0, 2 * kt + 1);
  }
  STAMP(10);
  float* stt = (float*)(smem + G1_ST);
  float mm[2], rs[2];
  f32x4 c4[NT];
  Frag F2;
  if (WF) __builtin_amdgcn_s_setprio(TPRIO);
  {
    auto ldt = [&](int j) {
      if (j < 3) ld1(F0, 7, 0, j);
      else if (j < 6) ld1(F2, 7, 1, j - 3);
      else if (j < NT + 5) ld1(F0, 7, 0, j - 3);
      else ld1(F2, 7, 1, j - NT - 2);
    };
#pragma unroll
    for (int i = 0; i < NM; ++i) {
      mm1(F1, NM - 1 - i);
      __builtin_amdgcn_sched_barrier(0);
      if (2 * i < 2 * NT + 4) ldt(2 * i);
      if (2 * i + 1 < 2 * NT + 4) ldt(2 * i + 1);
      __builtin_amdgcn_sched_barrier(0);
    }
#pragma unroll
    for (int i = NM - 1; i >= 2 * NT; --i) { mm1(F0, i); __builtin_amdgcn_sched_barrier(0); }
    if (!WF) { mm1(F0, 0); mm1(F0, 1); __builtin_amdgcn_sched_barrier(0); }
#pragma unroll
    for (int i = NM - 1; i >= 2 * NT; --i) { mm1(F2, i); __builtin_amdgcn_sched_barrier(0); }
  }
  if (!WF) {
#pragma unroll
    for (int i = 2; i < 2 * NT; ++i) { mm1(F0, i); __builtin_amdgcn_sched_barrier(0); }
  } else {
#pragma unroll
    for (int i = 0; i < TPRE; ++i) { mm1(F0, i); __builtin_amdgcn_sched_barrier(0); }
  }
  if (r15 == 0 && (!WF || wl < 3)) *(f32x4*)(stt + 256 + 16 * tix[0] + 4 * g) = accC;
  if (WF) {
    f32x2 sq, mean;
#pragma unroll
    for (int cc = 0; cc < 2; ++cc) {
      sq[cc] = (pp == 0) ? accG[cc][0] : (pp == 1) ? accG[cc][1] : (pp == 2) ? accG[cc][2] : accG[cc][3];
      mean[cc] = accS[cc][0];
    }
    mean *= (1.0f / D_);
    f32x2 var = sq * (1.0f / D_) - mean * mean;
    var = f32x2{fmaxf(var[0], 0.f), fmaxf(var[1], 0.f)};
    const uint32_t mb = cvt2(mean);
    uint32_t t = cvt2(var);
    t = cvt2(f32x2{bflo(t), bfhi(t)} + 1e-5f);
    t = cvt2(f32x2{__builtin_amdgcn_sqrtf(bflo(t)), __builtin_amdgcn_sqrtf(bfhi(t))});
    const float r0 = __builtin_amdgcn_rcpf(bflo(t)), r1 = __builtin_amdgcn_rcpf(bfhi(t));
    if (g == q) {
      *(f32x2*)(stt + 2 * (32 * wl + r15)) = f32x2{bflo(mb), r0};
      *(f32x2*)(stt + 2 * (32 * wl + 16 + r15)) = f32x2{bfhi(mb), r1};
    }
  } else {
    __builtin_amdgcn_sched_barrier(0);
#pragma unroll
    for (int i = 0; i < 2 * NT; ++i) { mm1(F2, i); __builtin_amdgcn_sched_barrier(0); }
  }
  STAMP(11);
  WAIT_LGKM0();
  RAW_BARRIER();
  __builtin_amdgcn_sched_barrier(0);
#pragma unroll
  for (int ci = 0; ci < NT; ++ci) c4[ci] = *(const f32x4*)(stt + 256 + 16 * tix[ci] + 4 * g);
#pragma unroll
  for (int cc = 0; cc < 2; ++cc) {
    const f32x2 t = *(const f32x2*)(stt + 2 * (32 * wl + 16 * cc + r15));
    mm[cc] = t[0]; rs[cc] = t[1];
  }
  if (WF) {
    __builtin_amdgcn_sched_barrier(0);
#pragma unroll
    for (int i = TPRE; i < 2 * NT; ++i) { mm1(F0, i); __builtin_amdgcn_sched_barrier(0); }
#pragma unroll
    for (int i = 0; i < 2 * NT; ++i) { mm1(F2, i); __builtin_amdgcn_sched_barrier(0); }
    __builtin_amdgcn_s_setprio(0);
  }
  STAMP(12);
  uint16_t* H1 = (uint16_t*)(ws + WS_H1);
#pragma unroll
  for (int ci = 0; ci < NT; ++ci) {
    uint32_t pk[2][2];
#pragma unroll
    for (int cc = 0; cc < 2; ++cc)
#pragma unroll
      for (int j2 = 0; j2 < 2; ++j2) {
        const f32x2 a = {acc[ci][cc][2 * j2], acc[ci][cc][2 * j2 + 1]};
        const f32x2 c = {c4[ci][2 * j2], c4[ci][2 * j2 + 1]};
        const uint32_t vb = cvt2((a - mm[cc] * c) * rs[cc]);
        const f32x2 v = {bflo(vb), bfhi(vb)};
        const f32x2 e = v * -1.4426950408889634f;
        const f32x2 dn = f32x2{__builtin_amdgcn_exp2f(e[0]), __builtin_amdgcn_exp2f(e[1])} + 1.0f;
        const f32x2 o = v * f32x2{__builtin_amdgcn_rcpf(dn[0]), __builtin_amdgcn_rcpf(dn[1])};
        pk[cc][j2] = cvt2(o);
      }
    swap16(pk[0][0], pk[1][0]);
    swap16(pk[0][1], pk[1][1]);
    uint16_t* p = H1 + (size_t)(lrow + 16 * (g & 1)) * F_ + f0 + 16 * tix[ci] + 8 * (g >> 1);
    st16<SM3>(p, u32x4{pk[0][0], pk[0][1], pk[1][0], pk[1][1]});
    __builtin_amdgcn_sched_barrier(0);
  }
  STAMP(13);
  STAMP_FLUSH(2);
}

extern "C" __global__ __launch_bounds__(640) void k3_gemm1(char* __restrict__ ws_) {
  const uint16_t* __restrict__ w1 = REC_PTR(const uint16_t*, 8);
  const uint16_t* __restrict__ w2 = REC_PTR(const uint16_t*, 9);
  char* __restrict__ ws = REC_PTR(char*, 10);
  extern __shared__ __attribute__((aligned(16))) char smem[];
  const int tid = threadIdx.x, lane = tid & 63;
  const int wv = __builtin_amdgcn_readfirstlane(tid >> 6);
  const int bid = blockIdx.x;
  const int xcd = bid & 7, idx = bid >> 3;
  const int lt = 4 * (xcd >> 1) + (idx & 3), ft = 8 * (xcd & 1) + (idx >> 2);
  const int l0 = lt * 128, f0 = ft * 112;
  if (wv >= 8) {
    const unsigned lbase = (unsigned)(size_t)(lds_char*)smem;
    const uint16_t* hT = (const uint16_t*)(ws + WS_HT);
    const int kt0 = ft >> 1;
    const int lw = wv - 8;
    STAMP_DECL
    unsigned hoff[8], woff[7];
#pragma unroll
    for (int i = 0; i < 8; ++i) {
      const int row = (8 * lw + i) * 4 + (lane >> 4);
      hoff[i] = 2u * (row * L_ + l0 + 8 * ((lane & 15) ^ (2 * (row & 7))));
    }
#pragma unroll
    for (int i = 0; i < 7; ++i) {
      const int nn = (7 * lw + i) * 64 + lane;
      const int r2 = nn / 14, ch = nn - r2 * 14;
      woff[i] = 2u * (r2 * F_ + f0 + 8 * ch);
    }
    auto stage = [&](int kt) {
      const unsigned sb = lbase + (kt % G1_NS) * G1_STAGE;
      const int ka = (kt0 + kt) & 7;
      const uint16_t* hs = hT + (size_t)ka * 64 * L_;
      const uint16_t* wsrc = w1 + (size_t)ka * 64 * F_;
#pragma unroll
      for (int i = 0; i < 8; ++i) {
        glds16s(hoff[i], hs, sb + (8 * lw + i) * 1024);
        if (i < 7) glds16s(woff[i], wsrc, sb + G1_HS + (7 * lw + i) * 1024);
      }
    };
    const char* src = (const char*)w2 + (size_t)idx * 57344 + lw * 28672;
#pragma unroll
    for (int s = 0; s < G1_LA; ++s) stage(s);
#pragma unroll
    for (int kt = -1; kt < 7; ++kt) {
      WAIT_VM_N(((kt + G1_LA < 7 ? kt + G1_LA : 7) - kt - 1) * 15 + g1_pfs(kt) - g1_pfs(kt - G1_LA));
      STAMP(kt + 2);
      RAW_BARRIER();
      if (kt + 1 + G1_LA < 8) stage(kt + 1 + G1_LA);
#pragma unroll
      for (int j = g1_pfs(kt); j < g1_pfs(kt + 1); ++j) glds16s(lane * 16u + j * 1024u, src, lbase + G1_DUMP);
    }
    STAMP(11);
    RAW_BARRIER();
#pragma unroll
    for (int j = g1_pfs(7); j < 28; ++j) glds16s(lane * 16u + j * 1024u, src, lbase + G1_DUMP);
    STAMP(12);
    WAIT_VM(0);
    STAMP(13);
    STAMP_FLUSH(2);
  } else if (wv < 4) k3_body<0>(ws, smem, lane, wv, l0, f0, ft);
  else k3_body<1>(ws, smem, lane, wv, l0, f0, ft);
}

constexpr int G2_HS = 64 * 256;
constexpr int G2_WS = 128 * 128;
constexpr int G2_STAGE = G2_HS + G2_WS;
constexpr int G2_NS = 5;
constexpr int G2_NK = F_ / 128;
constexpr int K4_LDS = G2_NS * G2_STAGE;

#define NLD 4
#define G2_LA 2
extern "C" __global__ __launch_bounds__(256 + 64 * NLD) void k4_gemm2(char* __restrict__ ws_) {
  const uint16_t* __restrict__ w2 = REC_PTR(const uint16_t*, 9);
  char* __restrict__ ws = REC_PTR(char*, 10);
  uint16_t* __restrict__ out = REC_PTR(uint16_t*, 11);
  extern __shared__ __attribute__((aligned(16))) char smem[];
  const int tid = threadIdx.x, lane = tid & 63;
  const int wv = __builtin_amdgcn_readfirstlane(tid >> 6);
  const int bid = blockIdx.x;
  const int xcd = bid & 7, idx = bid >> 3;
  STAMP_DECL
  const int lt = 4 * xcd + (idx & 3), dtile = idx >> 2;
  const int l0 = lt * 64, d0 = dtile * 64;
  const unsigned lbase = (unsigned)(size_t)(lds_char*)smem;
  const uint16_t* H1 = (const uint16_t*)(ws + WS_H1);
  if (wv >= 4) {
    constexpr int PP = 16 / NLD;
    const int p0 = (wv - 4) * PP;
    unsigned hoff[PP], woff[PP];
#pragma unroll
    for (int i = 0; i < PP; ++i) {
      const int row = (p0 + i) * 4 + (lane >> 4);
      hoff[i] = 2u * ((l0 + row) * F_ + 8 * ((lane & 15) ^ (row & 15)));
      const int r2 = (p0 + i) * 8 + (lane >> 3);
      const int gf = ((r2 >> 1) & 1) | (((r2 >> 3) & 1) << 1);
      woff[i] = 2u * (r2 * D_ + d0 + 8 * ((lane & 7) ^ (2 * gf)));
    }
    auto stage = [&](int kt) {
      const unsigned sb = lbase + (kt % G2_NS) * G2_STAGE + p0 * 1024;
#pragma unroll
      for (int i = 0; i < PP; ++i) {
        glds16s(woff[i], w2 + (size_t)kt * 128 * D_, sb + G2_HS + i * 1024);
        glds16s(hoff[i], H1 + kt * 128, sb + i * 1024);
      }
    };
#pragma unroll
    for (int s = 0; s < G2_LA; ++s) stage(s);
#pragma unroll
    for (int kt = -1; kt < G2_NK - 1; ++kt) {
      WAIT_VM_N(((kt + G2_LA < G2_NK - 1 ? kt + G2_LA : G2_NK - 1) - kt - 1) * 2 * PP);
      RAW_BARRIER();
      if (kt + 1 + G2_LA < G2_NK) stage(kt + 1 + G2_LA);
    }
    return;
  }
  const int g = lane >> 4, q = (lane >> 2) & 3, pp = lane & 3;
  const int wl = wv & 1, wd = wv >> 1;
  const int r15 = lane & 15;
  const size_t oo = (size_t)(l0 + 32 * wl + r15) * D_ + d0 + 32 * wd + 16 * (g & 1) + 8 * (g >> 1);
  u32x4 hres[2];
  hres[0] = *(const u32x4*)((const uint16_t*)(ws + WS_HR) + oo);
  hres[1] = *(const u32x4*)((const uint16_t*)(ws + WS_HR) + oo + 16 * D_);

  int hrd[4];
#pragma unroll
  for (int ks = 0; ks < 4; ++ks) hrd[ks] = (32 * wl + r15) * 256 + (((4 * ks + g) ^ r15) * 16);
  int wrd[2][2];
#pragma unroll
  for (int ci = 0; ci < 2; ++ci)
#pragma unroll
    for (int t = 0; t < 2; ++t) {
      const int row = 8 * g + 4 * t + q;
      const int gf = ((row >> 1) & 1) | (((row >> 3) & 1) << 1);
      wrd[ci][t] = G2_HS + row * 128 + (((2 * (2 * wd + ci)) ^ (2 * gf)) + (pp >> 1)) * 16 + 8 * (pp & 1);
    }
  f32x4 acc[2][2];
  acc[0][0] = acc[0][1] = acc[1][0] = acc[1][1] = f32x4{0, 0, 0, 0};

  struct Frag { bf16x8 h[2], w[2]; };
  auto ldfrag = [&](Frag& F, int kt, int ks) {
    const char* sb = smem + (kt % G2_NS) * G2_STAGE;
    F.h[0] = *(const bf16x8*)(sb + hrd[ks]);
    F.w[0] = cat8(tr_read(sb + wrd[0][0] + ks * 4096), tr_read(sb + wrd[0][1] + ks * 4096));
    F.w[1] = cat8(tr_read(sb + wrd[1][0] + ks * 4096), tr_read(sb + wrd[1][1] + ks * 4096));
    F.h[1] = *(const bf16x8*)(sb + hrd[ks] + 4096);
    __builtin_amdgcn_sched_barrier(0);
  };
  auto mma = [&](const Frag& F) {
#pragma unroll
    for (int a = 0; a < 2; ++a)
#pragma unroll
      for (int ci = 0; ci < 2; ++ci)
        acc[a][ci] = __builtin_amdgcn_mfma_f32_16x16x32_bf16(F.w[ci], F.h[a], acc[a][ci], 0, 0, 0);
    __builtin_amdgcn_sched_barrier(0);
  };
  Frag F0, F1, F2, F3;
  STAMP(1);
  RAW_BARRIER();
  STAMP(2);
  ldfrag(F0, 0, 0);
  ldfrag(F1, 0, 1);
#pragma unroll
  for (int kt = 0; kt < G2_NK; ++kt) {
    if ((kt & 1) == 0) STAMP(3 + kt / 2);
    ldfrag(F2, kt, 2);
    mma(F0);
    ldfrag(F3, kt, 3);
    mma(F1);
    if (kt < G2_NK - 1) {
      if (G2_LA == 4 && kt + 5 < G2_NK) WAIT_LGKM0();
      RAW_BARRIER();
      ldfrag(F0, kt + 1, 0);
    }
    mma(F2);
    if (kt < G2_NK - 1) ldfrag(F1, kt + 1, 1);
    mma(F3);
  }
  STAMP(10);
#pragma unroll
  for (int a = 0; a < 2; ++a) {
    uint32_t a0 = pack2(acc[a][0][0], acc[a][0][1]), a1 = pack2(acc[a][0][2], acc[a][0][3]);
    uint32_t b0 = pack2(acc[a][1][0], acc[a][1][1]), b1 = pack2(acc[a][1][2], acc[a][1][3]);
    swap16(a0, b0); swap16(a1, b1);
    const uint32_t v[4] = {a0, a1, b0, b1};
    u32x4 r;
#pragma unroll
    for (int j = 0; j < 4; ++j) r[j] = pack2(bflo(hres[a][j]) + bflo(v[j]), bfhi(hres[a][j]) + bfhi(v[j]));
    st16<SM4>(out + oo + a * 16 * D_, r);
  }
  STAMP(11);
  STAMP_FLUSH(3);
}

extern "C" void kernel_launch(void* const* d_in, const int* in_sizes, int n_in,
                              void* d_out, int out_size, void* d_ws, size_t ws_size,
                              hipStream_t stream) {
  const uint16_t* x = (const uint16_t*)d_in[0];
  const uint16_t* Are = (const uint16_t*)d_in[1];
  const uint16_t* Aim = (const uint16_t*)d_in[2];
  const uint16_t* Bre = (const uint16_t*)d_in[3];
  const uint16_t* Bim = (const uint16_t*)d_in[4];
  const uint16_t* Cre = (const uint16_t*)d_in[5];
  const uint16_t* Cim = (const uint16_t*)d_in[6];
  const uint16_t* logdt = (const uint16_t*)d_in[7];
  const uint16_t* Dskip = (const uint16_t*)d_in[8];
  const uint16_t* w1 = (const uint16_t*)d_in[10];
  const uint16_t* w2 = (const uint16_t*)d_in[11];
  char* ws = (char*)d_ws;
  (void)hipFuncSetAttribute((const void*)k1_prep, hipFuncAttributeMaxDynamicSharedMemorySize, K1_LDS);
  (void)hipFuncSetAttribute((const void*)k2_conv, hipFuncAttributeMaxDynamicSharedMemorySize, K2_LDS);
  (void)hipFuncSetAttribute((const void*)k3_gemm1, hipFuncAttributeMaxDynamicSharedMemorySize, K3_LDS);
  (void)hipFuncSetAttribute((const void*)k4_gemm2, hipFuncAttributeMaxDynamicSharedMemorySize, K4_LDS);
  k1_prep<<<256, 576, K1_LDS, stream>>>(x, Are, Aim, Bre, Bim, Cre, Cim, logdt, Dskip, ws, (const char*)d_out, (const char*)w1, (const char*)w2);
  k2_conv<<<D_ / 2, 768, K2_LDS, stream>>>(ws);
  k3_gemm1<<<256, 640, K3_LDS, stream>>>(ws);
  k4_gemm2<<<256, 256 + 64 * NLD, K4_LDS, stream>>>(ws);
}
```
